```python
import torch

def _softmax(x, axis=-1):
    return torch.softmax(x, dim=axis)
def _gelu(x):
    return torch.nn.functional.gelu(x, approximate="tanh")
def _silu(x):
    return torch.nn.functional.silu(x)
def _sigmoid(x):
    return torch.sigmoid(x)
def _layernorm(x, axis=-1, eps=1e-5):
    m = x.mean(dim=axis, keepdim=True)
    v = x.var(dim=axis, keepdim=True, unbiased=False)
    return (x - m) / torch.sqrt(v + eps)
def _scatter_add(tgt, idx, src, axis=0):
    return tgt.clone().index_add_(axis, idx, src)
def _segment_sum(src, seg, n):
    out = torch.zeros((n,) + src.shape[1:], dtype=src.dtype)
    return out.index_add_(0, seg, src)
def _pad(x, pw):
    flat = tuple(v for pair in reversed(pw) for v in pair)
    return torch.nn.functional.pad(x, flat)

kernel_name = 'gated_mix__grouped_kv_mqa__grouped_kv_mqa__prompt'

def setup_inputs():

    _w = (lambda a: (lambda t: t.to(torch.bfloat16) if t.is_floating_point() else t)(torch.as_tensor(a)))
    def _A_setup():
        B, S, D, H = 1, 2048, 640, 32
        Dh = D // H
        return {
            "x": _w((lambda *s: torch.randn(*s, dtype=torch.bfloat16) * 0.05)(B, S, D)),
            "wq": _w((lambda *s: torch.randn(*s, dtype=torch.bfloat16) * 0.8)(D, H * Dh)),
            "wk": _w((lambda *s: torch.randn(*s, dtype=torch.bfloat16) * 0.8)(D, Dh)),
            "wv": _w((lambda *s: torch.randn(*s, dtype=torch.bfloat16) * 0.05)(D, Dh)),
            "wo": _w((lambda *s: torch.randn(*s, dtype=torch.bfloat16) * 0.05)(H * Dh, D)),
        }
    _A_in = _A_setup()

    def _B_setup():
        B, S, D, H = 1, 2048, 640, 32
        Dh = D // H
        return {
            "x": _w((lambda *s: torch.randn(*s, dtype=torch.bfloat16) * 0.05)(B, S, D)),
            "wq": _w((lambda *s: torch.randn(*s, dtype=torch.bfloat16) * 0.8)(D, H * Dh)),
            "wk": _w((lambda *s: torch.randn(*s, dtype=torch.bfloat16) * 0.8)(D, Dh)),
            "wv": _w((lambda *s: torch.randn(*s, dtype=torch.bfloat16) * 0.05)(D, Dh)),
            "wo": _w((lambda *s: torch.randn(*s, dtype=torch.bfloat16) * 0.05)(H * Dh, D)),
        }
    _B_in = _B_setup()

    return {
        "A_x": _A_in["x"],
        "A_wq": _A_in["wq"],
        "A_wk": _A_in["wk"],
        "A_wv": _A_in["wv"],
        "A_wo": _A_in["wo"],
        "B_x": _B_in["x"],
        "B_wq": _B_in["wq"],
        "B_wk": _B_in["wk"],
        "B_wv": _B_in["wv"],
        "B_wo": _B_in["wo"],
        "gate_w": _w((lambda *s: torch.randn(*s, dtype=torch.bfloat16) * 0.05)(640) * 16.0),
    }

def reference(A_x, A_wq, A_wk, A_wv, A_wo, B_x, B_wq, B_wk, B_wv, B_wo, gate_w):
    def _A_ref(x, wq, wk, wv, wo):
        B, S, D = x.shape
        H = 32
        Dh = D // H
        q = (lambda x, *d: x.permute(*d))((x @ wq).reshape(B, S, H, Dh), 0, 2, 1, 3)
        k = x @ wk
        v = x @ wv
        scores = torch.einsum("bhqd,bkd->bhqk", q, k) * (Dh ** -0.5)
        p = _softmax(scores, axis=-1)
        o = torch.einsum("bhqk,bkd->bhqd", p, v)
        return (lambda x, *d: x.permute(*d))(o, 0, 2, 1, 3).reshape(B, S, H * Dh) @ wo

    def _B_ref(x, wq, wk, wv, wo):
        B, S, D = x.shape
        H = 32
        Dh = D // H
        q = (lambda x, *d: x.permute(*d))((x @ wq).reshape(B, S, H, Dh), 0, 2, 1, 3)
        k = x @ wk
        v = x @ wv
        scores = torch.einsum("bhqd,bkd->bhqk", q, k) * (Dh ** -0.5)
        p = _softmax(scores, axis=-1)
        o = torch.einsum("bhqk,bkd->bhqd", p, v)
        return (lambda x, *d: x.permute(*d))(o, 0, 2, 1, 3).reshape(B, S, H * Dh) @ wo

    x = A_x
    g = _sigmoid((x * gate_w).sum(-1, keepdims=True))
    a = _A_ref(x, A_wq, A_wk, A_wv, A_wo)
    b = _B_ref(x, B_wq, B_wk, B_wv, B_wo)
    return g * a + (1 - g) * b
```

```cpp
#include <hip/hip_runtime.h>
#include <hip/hip_bf16.h>
typedef __hip_bfloat16 bf16;
#define S 2048
#define D 640
#define H 32
#define DH 20
__device__ inline float b2f(bf16 v){ return __bfloat162float(v);}

__global__ void k_proj(const bf16* A, const bf16* B, float* C, int N, int K){
  int n = blockIdx.x*blockDim.x+threadIdx.x; int m = blockIdx.y;
  if(n>=N) return;
  float acc=0.f;
  for(int k=0;k<K;++k) acc += b2f(A[m*K+k])*b2f(B[k*N+n]);
  C[m*N+n]=acc;
}
__global__ void k_attn(const float* q, const float* k, const float* v, float* o){
  int row = blockIdx.x*blockDim.x+threadIdx.x;
  int t=row/H, h=row%H;
  float qr[DH];
  #pragma unroll
  for(int d=0;d<DH;++d) qr[d]=q[t*D+h*DH+d]*0.22360679775f;
  float m=-1e30f,l=0.f; float acc[DH];
  #pragma unroll
  for(int d=0;d<DH;++d) acc[d]=0.f;
  for(int j=0;j<S;++j){
    float s=0.f;
    #pragma unroll
    for(int d=0;d<DH;++d) s+=qr[d]*k[j*DH+d];
    float mn=fmaxf(m,s); float a=__expf(m-mn); float p=__expf(s-mn);
    l=l*a+p;
    #pragma unroll
    for(int d=0;d<DH;++d) acc[d]=acc[d]*a+p*v[j*DH+d];
    m=mn;
  }
  #pragma unroll
  for(int d=0;d<DH;++d) o[t*D+h*DH+d]=acc[d]/l;
}
__global__ void k_out(const float* oa, const float* ob, const bf16* woa, const bf16* wob, const bf16* x, const bf16* gw, bf16* out){
  int n = blockIdx.x*blockDim.x+threadIdx.x; int m = blockIdx.y;
  if(n>=D) return;
  float g=0.f; for(int k=0;k<D;++k) g+=b2f(x[m*D+k])*b2f(gw[k]);
  g=1.f/(1.f+__expf(-g));
  float a=0.f,b=0.f;
  for(int k=0;k<D;++k){ a+=oa[m*D+k]*b2f(woa[k*D+n]); b+=ob[m*D+k]*b2f(wob[k*D+n]); }
  out[m*D+n]=__float2bfloat16(g*a+(1.f-g)*b);
}
extern "C" void kernel_launch(void* const* d_in, const int* in_sizes, int n_in,
                              void* d_out, int out_size, void* d_ws, size_t ws_size,
                              hipStream_t stream) {
  const bf16* x=(const bf16*)d_in[0];
  float* ws=(float*)d_ws;
  float* o[2];
  for(int b=0;b<2;++b){
    const bf16* wq=(const bf16*)d_in[1+5*b], *wk=(const bf16*)d_in[2+5*b], *wv=(const bf16*)d_in[3+5*b];
    float* q=ws+(size_t)b*4*S*D; float* k=q+S*D; float* v=k+S*D; o[b]=v+S*D;
    k_proj<<<dim3(5,S),128,0,stream>>>(x,wq,q,D,D);
    k_proj<<<dim3(1,S),64,0,stream>>>(x,wk,k,DH,D);
    k_proj<<<dim3(1,S),64,0,stream>>>(x,wv,v,DH,D);
    k_attn<<<S*H/64,64,0,stream>>>(q,k,v,o[b]);
  }
  k_out<<<dim3(5,S),128,0,stream>>>(o[0],o[1],(const bf16*)d_in[4],(const bf16*)d_in[9],x,(const bf16*)d_in[10],(bf16*)d_out);
}
```

```cpp
#include <hip/hip_runtime.h>

typedef float f32x4 __attribute__((ext_vector_type(4)));
typedef float f32x2 __attribute__((ext_vector_type(2)));
typedef float f32x8 __attribute__((ext_vector_type(8)));
typedef float f32x16 __attribute__((ext_vector_type(16)));
typedef float f32x32 __attribute__((ext_vector_type(32)));
typedef unsigned u32x2 __attribute__((ext_vector_type(2)));
typedef unsigned u32x4 __attribute__((ext_vector_type(4)));
typedef unsigned u32x8 __attribute__((ext_vector_type(8)));
typedef unsigned u32x16 __attribute__((ext_vector_type(16)));
typedef short s16x4 __attribute__((ext_vector_type(4)));
typedef __bf16 bf16x2 __attribute__((ext_vector_type(2)));
typedef __bf16 bf16x8 __attribute__((ext_vector_type(8)));
typedef __bf16 bf16x16 __attribute__((ext_vector_type(16)));
typedef unsigned long long u64;
typedef __amdgpu_buffer_rsrc_t rsrc_t;
#define DI __device__ __forceinline__

constexpr unsigned WS_E = 0, WS_GF = 16384, WS_KVF = 32768, WS_KV = 65536, KV_BR = 196608, WS_W2F = 458752, WS_FO = 524288,
                   WS_W2H = 2097152, WS_W2T = 3407872, WS_OF = 3932160, WS_SIZE = 10u << 20, OOB = 0x40000000u;
constexpr unsigned L_XR = 0, L_QS = 40960, QSLOT = 24576, L_W = 114688, L_G = 140288, L_STG = 143360, L_KV0 = 125440, KVCH = 16896, L_OX = 40960, L_RED = 49152, L_T4 = 0, L_WT = 0, L_END = 163840;
constexpr unsigned PB = 5632;

DI rsrc_t mk(const void* p, unsigned n) { return __builtin_amdgcn_make_buffer_rsrc((void*)p, 0, n, 0x00020000); }
DI unsigned sel(bool c, unsigned a, unsigned b) { return c ? a : b; }
DI unsigned pk(float lo, float hi) { f32x2 v = {lo, hi}; return __builtin_bit_cast(unsigned, __builtin_convertvector(v, bf16x2)); }
DI unsigned vrow(unsigned lane) { const unsigned r = lane & 31; return (lane >> 5) * 384 + sel(r < 24, r, sel(r < 28, 23, 19)) * 16; }

DI float hi16(float v) { return __uint_as_float(__float_as_uint(v) & 0xffff0000u); }

DI void slow_pair(f32x16& oa, f32x16& ob, u32x4 qa, u32x4 qb, u32x2 q4, rsrc_t ws, unsigned kv, int lane) {
  float ma = -3.0e38f, mb = -3.0e38f, la = 0.f, lb = 0.f;
  for (int i = 0; i < 16; ++i) { oa[i] = 0.f; ob[i] = 0.f; }
  const unsigned av = vrow(lane);
#pragma unroll 1
  for (int kt = 0; kt < 64; ++kt) {
    const unsigned pb = kv + (kt >> 1) * PB, par = kt & 1;
    const u32x4 k16 = __builtin_amdgcn_raw_buffer_load_b128(ws, lane * 16, pb + 2048 + par * 1024, 16);
    const u32x4 v0 = __builtin_amdgcn_raw_buffer_load_b128(ws, av, pb + (par ? 4096 : 256), 16);
    const u32x4 v1 = __builtin_amdgcn_raw_buffer_load_b128(ws, av, pb + (par ? 4864 : 1280), 16);
    const u32x2 kf = __builtin_amdgcn_raw_buffer_load_b64(ws, (lane & 31) * 8, pb + par * 1024, 16);
    f32x32 z; for (int i = 0; i < 32; ++i) z[i] = 0.f;
    z = __builtin_amdgcn_mfma_f32_32x32x4bf16_1k(__builtin_bit_cast(s16x4, kf), __builtin_bit_cast(s16x4, q4), z, 0, 0, 0);
    f32x16 sa, sb; for (int i = 0; i < 16; ++i) { sa[i] = z[i]; sb[i] = z[16 + i]; }
    sa = __builtin_amdgcn_mfma_f32_32x32x16_bf16(__builtin_bit_cast(bf16x8, k16), __builtin_bit_cast(bf16x8, qa), sa, 0, 0, 0);
    sb = __builtin_amdgcn_mfma_f32_32x32x16_bf16(__builtin_bit_cast(bf16x8, k16), __builtin_bit_cast(bf16x8, qb), sb, 0, 0, 0);
    float na = ma, nb = mb;
    for (int i = 0; i < 16; ++i) { na = fmaxf(na, sa[i]); nb = fmaxf(nb, sb[i]); }
    na = fmaxf(na, __shfl_xor(na, 32)); nb = fmaxf(nb, __shfl_xor(nb, 32));
    const float fa = __builtin_amdgcn_exp2f(ma - na), fb = __builtin_amdgcn_exp2f(mb - nb);
    ma = na; mb = nb;
    f32x8 p0, p1, p2, p3;
    for (int i = 0; i < 8; ++i) {
      p0[i] = __builtin_amdgcn_exp2f(sa[i] - na); p1[i] = __builtin_amdgcn_exp2f(sa[8 + i] - na);
      p2[i] = __builtin_amdgcn_exp2f(sb[i] - nb); p3[i] = __builtin_amdgcn_exp2f(sb[8 + i] - nb);
    }
    for (int i = 0; i < 16; ++i) { oa[i] *= fa; ob[i] *= fb; }
    la *= fa; lb *= fb;
    for (int i = 0; i < 8; ++i) { la += hi16(p0[i]) + hi16(p1[i]); lb += hi16(p2[i]) + hi16(p3[i]); }
    const int idx = (int)0xddddddddu;
    oa = __builtin_amdgcn_smfmac_f32_32x32x32_bf16(__builtin_bit_cast(bf16x8, v0), __builtin_bit_cast(bf16x16, p0), oa, idx, 0, 0);
    oa = __builtin_amdgcn_smfmac_f32_32x32x32_bf16(__builtin_bit_cast(bf16x8, v1), __builtin_bit_cast(bf16x16, p1), oa, idx, 0, 0);
    ob = __builtin_amdgcn_smfmac_f32_32x32x32_bf16(__builtin_bit_cast(bf16x8, v0), __builtin_bit_cast(bf16x16, p2), ob, idx, 0, 0);
    ob = __builtin_amdgcn_smfmac_f32_32x32x32_bf16(__builtin_bit_cast(bf16x8, v1), __builtin_bit_cast(bf16x16, p3), ob, idx, 0, 0);
  }
  oa[10] = la + __shfl_xor(la, 32); ob[10] = lb + __shfl_xor(lb, 32);
}


__global__ void __launch_bounds__(512, 2)
mqa_kernel(const char*, const char*, const char*, const char*, const char*, const char*, const char*, const char*, const char*, const char*, char*, char*) {
  __shared__ __attribute__((aligned(1024))) char lds[L_END];
  u64 ax, aq, am, aoA; u32x8 ah;
  {
    unsigned o1, o2;
    asm volatile("s_load_dwordx8 s[76:83], %[ka], 0x40\n\ts_load_dwordx2 s[84:85], %[ka], 0x0\n\ts_and_b32 %[o1], %[bid], 1\n\ts_lshl_b32 %[o1], %[o1], 5\n\t"
                 "s_and_b32 %[o2], %[bid], 8\n\ts_add_u32 %[o2], %[o2], %[o1]\n\ts_load_dwordx2 s[88:89], %[ka], %[o2] offset:0x10\n\t"
                 "s_load_dwordx2 s[86:87], %[ka], %[o1] offset:0x8\n\ts_load_dwordx2 s[90:91], %[ka], 0x20"
                 : "=&{s[76:83]}"(ah), "=&{s[84:85]}"(ax), "=&{s[86:87]}"(aq), "=&{s[88:89]}"(am), "=&{s[90:91]}"(aoA), [o1] "=&s"(o1), [o2] "=&s"(o2)
                 : [ka] "s"(__builtin_amdgcn_kernarg_segment_ptr()), [bid] "s"(blockIdx.x) : "scc");
  }
#define KARG_WAIT() asm volatile("s_waitcnt lgkmcnt(0)" : "+{s[76:83]}"(ah), "+{s[84:85]}"(ax), "+{s[86:87]}"(aq), "+{s[88:89]}"(am), "+{s[90:91]}"(aoA))
#define KARG(lo, hi) ((char*)(((u64)(unsigned)__builtin_amdgcn_readfirstlane(hi) << 32) | (unsigned)__builtin_amdgcn_readfirstlane(lo)))
  u32x4 srdc;
  {
    unsigned po_;
    asm volatile("s_getpc_b64 s[72:73]\n\ts_and_b32 s72, s72, 0xffffff00\n\ts_and_b32 s73, s73, 0xffff\n\ts_mov_b32 s74, 0x6900\n\ts_mov_b32 s75, 0x20000\n\t"
                 "s_bfe_u32 m0, %[bid], %[bf]\n\tv_lshl_add_u32 %[po], %[tid], %[sh], m0\n\tv_lshlrev_b32 %[po], 7, %[po]\n\ts_mov_b32 m0, 0x27f00\n\ts_nop 0\n\t"
                 "buffer_load_dword %[po], s[72:75], 0 offen lds"
                 : [po] "=&v"(po_), "=&{s[72:75]}"(srdc) : [bid] "s"(blockIdx.x), [tid] "v"(threadIdx.x), [bf] "i"(65539), [sh] "i"(1) : "memory", "scc");
    __builtin_amdgcn_sched_barrier(0);
  }
  const unsigned lane = threadIdx.x & 63;
  const unsigned w8 = __builtin_amdgcn_readfirstlane(threadIdx.x >> 6), w = w8 & 3;
  const unsigned bid = blockIdx.x, tb = ((bid >> 1) & 3) * 4 + (bid >> 6), b = bid & 1, hg = (bid >> 3) & 7, col = b * 8 + hg;
  const unsigned wid = tb * 16 + col, wkv = hg >> 1, isv = hg & 1;
  const unsigned c0 = ((bid >> 3) + tb * 3) % 10;
  const unsigned vlin = lane * 16, q = (lane >> 2) & 3, p = lane & 3, h = lane >> 5, g4 = lane >> 4;
  unsigned t0, t1;

  if (w8 >= 4) {
    const unsigned vrow8 = (8 * w + (lane >> 3)) * 1280 + (((lane & 7) ^ ((4 * w + g4) & 7)) * 16);
    unsigned vxr = vrow8 + wkv * 40960, sow = w * 3072, m0x = L_XR + w * 1024, xo = tb * 128 * 1280;
    const bool isg = (w == 1) | (w == 2);
    unsigned soe = sel(isg, (w - 1) * 1024, 12288), m0e = sel(isg, L_G, L_W) + soe;
    asm volatile("" :: "v"(vxr), "v"(vlin), "s"(sow), "s"(m0e), "s"(soe), "s"(m0x), "s"(xo), "s"(c0));
    KARG_WAIT();
    char* const ws = KARG(ah[6], ah[7]);
    const rsrc_t srdx = mk(KARG((unsigned)ax, (unsigned)(ax >> 32)) + xo, 128 * 1280);
    {
      const char* wm = KARG((unsigned)am, (unsigned)(am >> 32));
      const rsrc_t srdw = mk(wm, 25600), srde = mk(isg ? KARG(ah[2], ah[3]) : wm, isg ? 1280 : 25600);
      asm volatile(
        "s_mov_b32 %[t1], %[r5]\n\t"
        "s_add_u32 m0, %[sow], 114688\n\t"
        "s_add_u32 %[t0], %[sow], 0\n\t"
        "buffer_load_dwordx4 %[vlin], %[srdw], %[t0] offen offset:0 lds\n\t"
        "buffer_load_dwordx4 %[vlin], %[srdw], %[t0] offen offset:1024 lds\n\t"
        "buffer_load_dwordx4 %[vlin], %[srdw], %[t0] offen offset:2048 lds\n\t"
        "s_mov_b32 m0, %[m0e]\n\t"
        "s_nop 0\n\t"
        "buffer_load_dwordx4 %[vlin], %[srde], %[soe] offen lds\n\t"
        "s_lshl_b32 %[t0], %[t1], 12\n\t"
        "s_add_u32 m0, %[t0], %[m0x]\n\t"
        "s_lshl_b32 %[t0], %[t1], 7\n\t"
        "buffer_load_dwordx4 %[vxr], %[srdx], %[t0] offen lds\n\t"
        "s_add_u32 %[t1], %[t1], 1\n\t"
        "s_cmp_eq_u32 %[t1], 10\n\t"
        "s_cselect_b32 %[t1], 0, %[t1]\n\t"
        "s_lshl_b32 %[t0], %[t1], 12\n\t"
        "s_add_u32 m0, %[t0], %[m0x]\n\t"
        "s_lshl_b32 %[t0], %[t1], 7\n\t"
        "buffer_load_dwordx4 %[vxr], %[srdx], %[t0] offen lds\n\t"
        "s_add_u32 %[t1], %[t1], 1\n\t"
        "s_cmp_eq_u32 %[t1], 10\n\t"
        "s_cselect_b32 %[t1], 0, %[t1]\n\t"
        "s_lshl_b32 %[t0], %[t1], 12\n\t"
        "s_add_u32 m0, %[t0], %[m0x]\n\t"
        "s_lshl_b32 %[t0], %[t1], 7\n\t"
        "buffer_load_dwordx4 %[vxr], %[srdx], %[t0] offen lds\n\t"
        "s_add_u32 %[t1], %[t1], 1\n\t"
        "s_cmp_eq_u32 %[t1], 10\n\t"
        "s_cselect_b32 %[t1], 0, %[t1]\n\t"
        "s_lshl_b32 %[t0], %[t1], 12\n\t"
        "s_add_u32 m0, %[t0], %[m0x]\n\t"
        "s_lshl_b32 %[t0], %[t1], 7\n\t"
        "buffer_load_dwordx4 %[vxr], %[srdx], %[t0] offen lds\n\t"
        "s_add_u32 %[t1], %[t1], 1\n\t"
        "s_cmp_eq_u32 %[t1], 10\n\t"
        "s_cselect_b32 %[t1], 0, %[t1]\n\t"
        "s_lshl_b32 %[t0], %[t1], 12\n\t"
        "s_add_u32 m0, %[t0], %[m0x]\n\t"
        "s_lshl_b32 %[t0], %[t1], 7\n\t"
        "buffer_load_dwordx4 %[vxr], %[srdx], %[t0] offen lds\n\t"
        "s_add_u32 %[t1], %[t1], 1\n\t"
        "s_cmp_eq_u32 %[t1], 10\n\t"
        "s_cselect_b32 %[t1], 0, %[t1]\n\t"
        "s_add_u32 m0, %[sow], 128000\n\t"
        "s_add_u32 %[t0], %[sow], 13312\n\t"
        "buffer_load_dwordx4 %[vlin], %[srdw], %[t0] offen offset:0 lds\n\t"
        "buffer_load_dwordx4 %[vlin], %[srdw], %[t0] offen offset:1024 lds\n\t"
        "buffer_load_dwordx4 %[vlin], %[srdw], %[t0] offen offset:2048 lds\n\t"
        "s_lshl_b32 %[t0], %[t1], 12\n\t"
        "s_add_u32 m0, %[t0], %[m0x]\n\t"
        "s_lshl_b32 %[t0], %[t1], 7\n\t"
        "buffer_load_dwordx4 %[vxr], %[srdx], %[t0] offen lds\n\t"
        "s_add_u32 %[t1], %[t1], 1\n\t"
        "s_cmp_eq_u32 %[t1], 10\n\t"
        "s_cselect_b32 %[t1], 0, %[t1]\n\t"
        "s_lshl_b32 %[t0], %[t1], 12\n\t"
        "s_add_u32 m0, %[t0], %[m0x]\n\t"
        "s_lshl_b32 %[t0], %[t1], 7\n\t"
        "buffer_load_dwordx4 %[vxr], %[srdx], %[t0] offen lds\n\t"
        "s_add_u32 %[t1], %[t1], 1\n\t"
        "s_cmp_eq_u32 %[t1], 10\n\t"
        "s_cselect_b32 %[t1], 0, %[t1]\n\t"
        "s_lshl_b32 %[t0], %[t1], 12\n\t"
        "s_add_u32 m0, %[t0], %[m0x]\n\t"
        "s_lshl_b32 %[t0], %[t1], 7\n\t"
        "buffer_load_dwordx4 %[vxr], %[srdx], %[t0] offen lds\n\t"
        "s_add_u32 %[t1], %[t1], 1\n\t"
        "s_cmp_eq_u32 %[t1], 10\n\t"
        "s_cselect_b32 %[t1], 0, %[t1]\n\t"
        "s_lshl_b32 %[t0], %[t1], 12\n\t"
        "s_add_u32 m0, %[t0], %[m0x]\n\t"
        "s_lshl_b32 %[t0], %[t1], 7\n\t"
        "buffer_load_dwordx4 %[vxr], %[srdx], %[t0] offen lds\n\t"
        "s_add_u32 %[t1], %[t1], 1\n\t"
        "s_cmp_eq_u32 %[t1], 10\n\t"
        "s_cselect_b32 %[t1], 0, %[t1]\n\t"
        "s_lshl_b32 %[t0], %[t1], 12\n\t"
        "s_add_u32 m0, %[t0], %[m0x]\n\t"
        "s_lshl_b32 %[t0], %[t1], 7\n\t"
        "buffer_load_dwordx4 %[vxr], %[srdx], %[t0] offen lds\n\t"
        "s_add_u32 %[t1], %[t1], 1\n\t"
        "s_cmp_eq_u32 %[t1], 10\n\t"
        "s_cselect_b32 %[t1], 0, %[t1]\n\t"
        : [t0] "=&s"(t0), [t1] "=&s"(t1)
        : [vxr] "v"(vxr), [vlin] "v"(vlin), [srdx] "s"(srdx), [srdw] "s"(srdw), [srde] "s"(srde), [sow] "s"(sow), [m0e] "s"(m0e), [soe] "s"(soe),
          [m0x] "s"(m0x), [r5] "s"(c0)
        : "memory", "scc");
    }
    const rsrc_t srdws = mk(ws, WS_SIZE), srdout = mk(KARG(ah[4], ah[5]), 2048 * 1280);
    u64 tag, p0;
    unsigned it, c = c0, w2 = w, bid2 = bid;
    u32x8 in, in2, in3; u32x2 in0;
    {
      in[0] = WS_KVF + b * 8192 + lane * 128;
      for (unsigned j = 0; j < 3; ++j) in[1 + j] = vrow8 + (j + (j >= wkv)) * 40960;
      for (unsigned j = 0; j < 3; ++j) {
        const unsigned n = j * 64 + lane, rr = (n * 5462) >> 16, ch = n - 12 * rr - ((rr >> 2) & 2);
        in[4 + j] = sel(ch < 10, (16 * w + rr) * 1280 + ch * 16 + hg * 160 - 1024 * j, OOB);
      }
      in[7] = vlin;
      const rsrc_t srdq = mk(KARG((unsigned)aq, (unsigned)(aq >> 32)), 640 * 1280);
      const unsigned hw1k = w * 1024, hw3k = w * 3072, skv = WS_KV + b * KV_BR + hw1k;
      asm volatile(
        "s_load_dwordx2 s[68:69], %[wsp], 0x0\n\t"
        "s_add_u32 m0, %[hw1k], 40960\n\t"
        "s_lshl_b32 %[t0], %[c], 7\n\t"
        "buffer_load_dwordx4 v233, %[srdx], %[t0] offen offset:0 lds\n\t"
        "s_add_u32 m0, %[hw1k], 45056\n\t"
        "s_lshl_b32 %[t0], %[c], 7\n\t"
        "buffer_load_dwordx4 v234, %[srdx], %[t0] offen offset:0 lds\n\t"
        "s_add_u32 m0, %[hw1k], 49152\n\t"
        "s_lshl_b32 %[t0], %[c], 7\n\t"
        "buffer_load_dwordx4 v235, %[srdx], %[t0] offen offset:0 lds\n\t"
        "s_add_u32 m0, %[hw3k], 53248\n\t"
        "s_mul_i32 %[t0], %[c], 81920\n\t"
        "buffer_load_dwordx4 v236, %[srdq], %[t0] offen offset:0 lds\n\t"
        "buffer_load_dwordx4 v237, %[srdq], %[t0] offen offset:1024 lds\n\t"
        "buffer_load_dwordx4 v238, %[srdq], %[t0] offen offset:2048 lds\n\t"
        "s_add_u32 %[c], %[c], 1\n\t"
        "s_cmp_eq_u32 %[c], 10\n\t"
        "s_cselect_b32 %[c], 0, %[c]\n\t"
        "s_add_u32 m0, %[hw1k], 65536\n\t"
        "s_lshl_b32 %[t0], %[c], 7\n\t"
        "buffer_load_dwordx4 v233, %[srdx], %[t0] offen offset:0 lds\n\t"
        "s_add_u32 m0, %[hw1k], 69632\n\t"
        "s_lshl_b32 %[t0], %[c], 7\n\t"
        "buffer_load_dwordx4 v234, %[srdx], %[t0] offen offset:0 lds\n\t"
        "s_waitcnt lgkmcnt(0)\n\t"
        "s_add_u32 s68, s68, 1\n\t"
        "s_addc_u32 s69, s69, 0\n\t"
        "s_waitcnt vmcnt(8)\n\t"
        "s_barrier\n\t"
        "v_add_u32 v224, %[hw3k], v239\n\t"
        "v_add_u32 v224, 102400, v224\n\t"
        "ds_read_b128 v[240:243], v224\n\t"
        "ds_read_b128 v[244:247], v224 offset:1024\n\t"
        "ds_read_b128 v[248:251], v224 offset:2048\n\t"
        "v_lshrrev_b32 v253, 1, v239\n\t"
        "v_lshrrev_b32 v252, 9, v239\n\t"
        "v_mul_u32_u24 v252, 9728, v252\n\t"
        "v_add_u32 v252, v252, v239\n\t"
        "v_add_u32 v252, 2048, v252\n\t"
        "s_lshr_b32 %[t1], %[hw1k], 8\n\t"
        "s_add_u32 %[t1], %[t1], 163328\n\t"
        "v_mov_b32 v230, %[t1]\n\t"
        "v_mov_b32 v231, 1\n\t"
        "v_bfe_u32 v255, v239, 4, 2\n\t"
        "v_lshlrev_b32 v255, 2, v255\n\t"
        "v_add_u32 v255, 163328, v255\n\t"
        "s_add_u32 m0, %[hw1k], 73728\n\t"
        "s_lshl_b32 %[t0], %[c], 7\n\t"
        "buffer_load_dwordx4 v235, %[srdx], %[t0] offen offset:0 lds\n\t"
        "s_add_u32 m0, %[hw3k], 77824\n\t"
        "s_mul_i32 %[t0], %[c], 81920\n\t"
        "buffer_load_dwordx4 v236, %[srdq], %[t0] offen offset:0 lds\n\t"
        "buffer_load_dwordx4 v237, %[srdq], %[t0] offen offset:1024 lds\n\t"
        "buffer_load_dwordx4 v238, %[srdq], %[t0] offen offset:2048 lds\n\t"
        "s_add_u32 %[c], %[c], 1\n\t"
        "s_cmp_eq_u32 %[c], 10\n\t"
        "s_cselect_b32 %[c], 0, %[c]\n\t"
        "s_add_u32 m0, %[hw1k], 90112\n\t"
        "s_lshl_b32 %[t0], %[c], 7\n\t"
        "buffer_load_dwordx4 v233, %[srdx], %[t0] offen offset:0 lds\n\t"
        "s_add_u32 m0, %[hw1k], 94208\n\t"
        "s_lshl_b32 %[t0], %[c], 7\n\t"
        "buffer_load_dwordx4 v234, %[srdx], %[t0] offen offset:0 lds\n\t"
        "s_add_u32 m0, %[hw1k], 98304\n\t"
        "s_lshl_b32 %[t0], %[c], 7\n\t"
        "buffer_load_dwordx4 v235, %[srdx], %[t0] offen offset:0 lds\n\t"
        "s_waitcnt lgkmcnt(0)\n\t"
        "v_sub_u32 v254, v246, v253\n\t"
        "v_sub_u32 v253, v244, v253\n\t"
        "s_add_u32 m0, %[hw3k], 102400\n\t"
        "s_mul_i32 %[t0], %[c], 81920\n\t"
        "buffer_load_dwordx4 v236, %[srdq], %[t0] offen offset:0 lds\n\t"
        "buffer_load_dwordx4 v237, %[srdq], %[t0] offen offset:1024 lds\n\t"
        "buffer_load_dwordx4 v238, %[srdq], %[t0] offen offset:2048 lds\n\t"
        "s_add_u32 %[c], %[c], 1\n\t"
        "s_cmp_eq_u32 %[c], 10\n\t"
        "s_cselect_b32 %[c], 0, %[c]\n\t"
        "s_waitcnt vmcnt(12)\n\t"
        "s_barrier\n\t"
        "s_add_u32 m0, %[hw1k], 114688\n\t"
        "s_lshl_b32 %[t0], %[c], 7\n\t"
        "buffer_load_dwordx4 v233, %[srdx], %[t0] offen offset:0 lds\n\t"
        "s_add_u32 m0, %[hw1k], 118784\n\t"
        "s_lshl_b32 %[t0], %[c], 7\n\t"
        "buffer_load_dwordx4 v234, %[srdx], %[t0] offen offset:0 lds\n\t"
        "s_add_u32 m0, %[hw1k], 122880\n\t"
        "s_lshl_b32 %[t0], %[c], 7\n\t"
        "buffer_load_dwordx4 v235, %[srdx], %[t0] offen offset:0 lds\n\t"
        "s_add_u32 m0, %[hw3k], 126976\n\t"
        "s_mul_i32 %[t0], %[c], 81920\n\t"
        "buffer_load_dwordx4 v236, %[srdq], %[t0] offen offset:0 lds\n\t"
        "buffer_load_dwordx4 v237, %[srdq], %[t0] offen offset:1024 lds\n\t"
        "buffer_load_dwordx4 v238, %[srdq], %[t0] offen offset:2048 lds\n\t"
        "s_add_u32 %[c], %[c], 1\n\t"
        "s_cmp_eq_u32 %[c], 10\n\t"
        "s_cselect_b32 %[c], 0, %[c]\n\t"
        "s_waitcnt vmcnt(12)\n\t"
        "s_barrier\n\t"
        "s_add_u32 m0, %[hw1k], 40960\n\t"
        "s_lshl_b32 %[t0], %[c], 7\n\t"
        "buffer_load_dwordx4 v233, %[srdx], %[t0] offen offset:0 lds\n\t"
        "s_add_u32 m0, %[hw1k], 45056\n\t"
        "s_lshl_b32 %[t0], %[c], 7\n\t"
        "buffer_load_dwordx4 v234, %[srdx], %[t0] offen offset:0 lds\n\t"
        "s_add_u32 m0, %[hw1k], 49152\n\t"
        "s_lshl_b32 %[t0], %[c], 7\n\t"
        "buffer_load_dwordx4 v235, %[srdx], %[t0] offen offset:0 lds\n\t"
        "s_add_u32 m0, %[hw3k], 53248\n\t"
        "s_mul_i32 %[t0], %[c], 81920\n\t"
        "buffer_load_dwordx4 v236, %[srdq], %[t0] offen offset:0 lds\n\t"
        "buffer_load_dwordx4 v237, %[srdq], %[t0] offen offset:1024 lds\n\t"
        "s_waitcnt vmcnt(11)\n\t"
        "s_barrier\n\t"
        "buffer_load_dwordx4 v238, %[srdq], %[t0] offen offset:2048 lds\n\t"
        "s_add_u32 %[c], %[c], 1\n\t"
        "s_cmp_eq_u32 %[c], 10\n\t"
        "s_cselect_b32 %[c], 0, %[c]\n\t"
        "s_add_u32 m0, %[hw1k], 65536\n\t"
        "s_lshl_b32 %[t0], %[c], 7\n\t"
        "buffer_load_dwordx4 v233, %[srdx], %[t0] offen offset:0 lds\n\t"
        "s_add_u32 m0, %[hw1k], 69632\n\t"
        "s_lshl_b32 %[t0], %[c], 7\n\t"
        "buffer_load_dwordx4 v234, %[srdx], %[t0] offen offset:0 lds\n\t"
        "s_add_u32 m0, %[hw1k], 73728\n\t"
        "s_lshl_b32 %[t0], %[c], 7\n\t"
        "buffer_load_dwordx4 v235, %[srdx], %[t0] offen offset:0 lds\n\t"
        "s_add_u32 m0, %[hw3k], 77824\n\t"
        "s_mul_i32 %[t0], %[c], 81920\n\t"
        "buffer_load_dwordx4 v236, %[srdq], %[t0] offen offset:0 lds\n\t"
        "s_waitcnt vmcnt(10)\n\t"
        "s_barrier\n\t"
        "buffer_load_dwordx4 v237, %[srdq], %[t0] offen offset:1024 lds\n\t"
        "buffer_load_dwordx4 v238, %[srdq], %[t0] offen offset:2048 lds\n\t"
        "s_add_u32 %[c], %[c], 1\n\t"
        "s_cmp_eq_u32 %[c], 10\n\t"
        "s_cselect_b32 %[c], 0, %[c]\n\t"
        "s_add_u32 m0, %[hw1k], 90112\n\t"
        "s_lshl_b32 %[t0], %[c], 7\n\t"
        "buffer_load_dwordx4 v233, %[srdx], %[t0] offen offset:0 lds\n\t"
        "s_add_u32 m0, %[hw1k], 94208\n\t"
        "s_lshl_b32 %[t0], %[c], 7\n\t"
        "buffer_load_dwordx4 v234, %[srdx], %[t0] offen offset:0 lds\n\t"
        "s_add_u32 m0, %[hw1k], 98304\n\t"
        "s_lshl_b32 %[t0], %[c], 7\n\t"
        "buffer_load_dwordx4 v235, %[srdx], %[t0] offen offset:0 lds\n\t"
        "s_waitcnt vmcnt(9)\n\t"
        "s_barrier\n\t"
        "s_add_u32 m0, %[hw3k], 102400\n\t"
        "s_mul_i32 %[t0], %[c], 81920\n\t"
        "buffer_load_dwordx4 v236, %[srdq], %[t0] offen offset:0 lds\n\t"
        "buffer_load_dwordx4 v237, %[srdq], %[t0] offen offset:1024 lds\n\t"
        "buffer_load_dwordx4 v238, %[srdq], %[t0] offen offset:2048 lds\n\t"
        "s_add_u32 %[c], %[c], 1\n\t"
        "s_cmp_eq_u32 %[c], 10\n\t"
        "s_cselect_b32 %[c], 0, %[c]\n\t"
        "s_add_u32 m0, %[hw1k], 114688\n\t"
        "s_lshl_b32 %[t0], %[c], 7\n\t"
        "buffer_load_dwordx4 v233, %[srdx], %[t0] offen offset:0 lds\n\t"
        "s_add_u32 m0, %[hw1k], 118784\n\t"
        "s_lshl_b32 %[t0], %[c], 7\n\t"
        "buffer_load_dwordx4 v234, %[srdx], %[t0] offen offset:0 lds\n\t"
        "s_waitcnt vmcnt(8)\n\t"
        "s_barrier\n\t"
        "s_add_u32 m0, %[hw1k], 122880\n\t"
        "s_lshl_b32 %[t0], %[c], 7\n\t"
        "buffer_load_dwordx4 v235, %[srdx], %[t0] offen offset:0 lds\n\t"
        "s_add_u32 m0, %[hw3k], 126976\n\t"
        "s_mul_i32 %[t0], %[c], 81920\n\t"
        "buffer_load_dwordx4 v236, %[srdq], %[t0] offen offset:0 lds\n\t"
        "buffer_load_dwordx4 v237, %[srdq], %[t0] offen offset:1024 lds\n\t"
        "buffer_load_dwordx4 v238, %[srdq], %[t0] offen offset:2048 lds\n\t"
        "s_add_u32 %[c], %[c], 1\n\t"
        "s_cmp_eq_u32 %[c], 10\n\t"
        "s_cselect_b32 %[c], 0, %[c]\n\t"
        "s_add_u32 m0, %[hw1k], 40960\n\t"
        "s_lshl_b32 %[t0], %[c], 7\n\t"
        "buffer_load_dwordx4 v233, %[srdx], %[t0] offen offset:0 lds\n\t"
        "s_waitcnt vmcnt(7)\n\t"
        "s_barrier\n\t"
        "s_add_u32 m0, %[hw1k], 45056\n\t"
        "s_lshl_b32 %[t0], %[c], 7\n\t"
        "buffer_load_dwordx4 v234, %[srdx], %[t0] offen offset:0 lds\n\t"
        "s_add_u32 m0, %[hw1k], 49152\n\t"
        "s_lshl_b32 %[t0], %[c], 7\n\t"
        "buffer_load_dwordx4 v235, %[srdx], %[t0] offen offset:0 lds\n\t"
        "s_add_u32 m0, %[hw3k], 53248\n\t"
        "s_mul_i32 %[t0], %[c], 81920\n\t"
        "buffer_load_dwordx4 v236, %[srdq], %[t0] offen offset:0 lds\n\t"
        "buffer_load_dwordx4 v237, %[srdq], %[t0] offen offset:1024 lds\n\t"
        "buffer_load_dwordx4 v238, %[srdq], %[t0] offen offset:2048 lds\n\t"
        "s_add_u32 %[c], %[c], 1\n\t"
        "s_cmp_eq_u32 %[c], 10\n\t"
        "s_cselect_b32 %[c], 0, %[c]\n\t"
        "s_waitcnt vmcnt(6)\n\t"
        "s_barrier\n\t"
        "buffer_load_dwordx2 v[224:225], v245, %[srdws], 0 offen sc1\n\t"
        "s_add_u32 m0, %[hw1k], 65536\n\t"
        "s_lshl_b32 %[t0], %[c], 7\n\t"
        "buffer_load_dwordx4 v233, %[srdx], %[t0] offen offset:0 lds\n\t"
        "s_add_u32 m0, %[hw1k], 69632\n\t"
        "s_lshl_b32 %[t0], %[c], 7\n\t"
        "buffer_load_dwordx4 v234, %[srdx], %[t0] offen offset:0 lds\n\t"
        "s_add_u32 m0, %[hw1k], 73728\n\t"
        "s_lshl_b32 %[t0], %[c], 7\n\t"
        "buffer_load_dwordx4 v235, %[srdx], %[t0] offen offset:0 lds\n\t"
        "s_add_u32 m0, %[hw3k], 77824\n\t"
        "s_mul_i32 %[t0], %[c], 81920\n\t"
        "buffer_load_dwordx4 v236, %[srdq], %[t0] offen offset:0 lds\n\t"
        "buffer_load_dwordx4 v237, %[srdq], %[t0] offen offset:1024 lds\n\t"
        "buffer_load_dwordx4 v238, %[srdq], %[t0] offen offset:2048 lds\n\t"
        "s_waitcnt vmcnt(6)\n\t"
        "s_barrier\n\t"
        "s_mov_b64 %[p0], 0xffff\n\t"
        "v_cmp_ne_u64 vcc, s[68:69], v[224:225]\n\t"
        "s_and_b64 vcc, vcc, %[p0]\n\t"
        "s_cselect_b32 %[c], 1, 0\n\t"
        "s_cbranch_scc0 2f\n\t"
        "s_waitcnt vmcnt(0)\n\t"
        "s_barrier\n\t"
        "s_mov_b32 %[it], 65536\n\t"
        "3:\n\t"
        "buffer_load_dwordx2 v[224:225], v245, %[srdws], 0 offen sc1\n\t"
        "s_waitcnt vmcnt(0)\n\t"
        "v_cmp_ne_u64 vcc, s[68:69], v[224:225]\n\t"
        "s_and_b64 vcc, vcc, %[p0]\n\t"
        "s_cbranch_scc0 4f\n\t"
        "s_sleep 4\n\t"
        "s_sub_u32 %[it], %[it], 1\n\t"
        "s_cmp_lg_u32 %[it], 0\n\t"
        "s_cbranch_scc1 3b\n\t"
        "4:\n\t"
        "2:\n\t"
        "s_add_u32 %[t0], %[skv], 0\n\t"
        "s_add_u32 m0, %[hw1k], 125440\n\t"
        "s_mov_b32 %[t1], %[hw1k]\n\t"
        "9:\n\t"
        "buffer_load_dwordx4 v239, %[srdws], %[t0] offen sc1 lds\n\t"
        "s_add_u32 m0, m0, 4096\n\t"
        "s_add_u32 %[t0], %[t0], 4096\n\t"
        "s_add_u32 %[t1], %[t1], 4096\n\t"
        "s_cmp_lt_u32 %[t1], 21504\n\t"
        "s_cbranch_scc1 9b\n\t"
        "s_cmp_lg_u32 %[c], 0\n\t"
        "s_cbranch_scc1 6f\n\t"
        "s_waitcnt vmcnt(5)\n\t"
        "s_barrier\n\t"
        "6:\n\t"
        "buffer_load_dwordx2 v[224:225], v232, %[srdws], 0 offen sc1\n\t"
        "buffer_load_dwordx2 v[226:227], v232, %[srdws], 0 offen offset:64 sc1\n\t"
        "s_waitcnt vmcnt(2)\n\t"
        "s_barrier\n\t"
        "s_waitcnt vmcnt(0)\n\t"
        "s_setprio 3\n\t"
        "v_cmp_ne_u64 vcc, s[68:69], v[224:225]\n\t"
        "v_cmp_ne_u64 %[p0], s[68:69], v[226:227]\n\t"
        "s_setprio 0\n\t"
        "s_or_b64 vcc, vcc, %[p0]\n\t"
        "s_cbranch_scc0 11f\n\t"
        "s_mov_b32 %[it], 65536\n\t"
        "10:\n\t"
        "buffer_load_dwordx2 v[224:225], v232, %[srdws], 0 offen sc1\n\t"
        "buffer_load_dwordx2 v[226:227], v232, %[srdws], 0 offen offset:64 sc1\n\t"
        "s_waitcnt vmcnt(0)\n\t"
        "s_setprio 3\n\t"
        "v_cmp_ne_u64 vcc, s[68:69], v[224:225]\n\t"
        "v_cmp_ne_u64 %[p0], s[68:69], v[226:227]\n\t"
        "s_setprio 0\n\t"
        "s_or_b64 vcc, vcc, %[p0]\n\t"
        "s_cbranch_scc0 11f\n\t"
        "s_sleep 4\n\t"
        "s_sub_u32 %[it], %[it], 1\n\t"
        "s_cmp_lg_u32 %[it], 0\n\t"
        "s_cbranch_scc1 10b\n\t"
        "11:\n\t"
        "s_add_u32 %[t0], %[skv], 21504\n\t"
        "s_add_u32 m0, %[hw1k], 146944\n\t"
        "s_mov_b32 %[t1], %[hw1k]\n\t"
        "9:\n\t"
        "buffer_load_dwordx4 v239, %[srdws], %[t0] offen sc1 lds\n\t"
        "s_add_u32 m0, m0, 4096\n\t"
        "s_add_u32 %[t0], %[t0], 4096\n\t"
        "s_add_u32 %[t1], %[t1], 4096\n\t"
        "s_cmp_lt_u32 %[t1], 16384\n\t"
        "s_cbranch_scc1 9b\n\t"
        "s_waitcnt vmcnt(0)\n\t"
        "s_mov_b64 exec, 1\n\t"
        "ds_add_u32 v230, v231\n\t"
        "s_mov_b64 exec, -1\n\t"
        "s_add_u32 %[t0], %[skv], 37888\n\t"
        "s_add_u32 m0, %[hw1k], 40960\n\t"
        "s_mov_b32 %[t1], %[hw1k]\n\t"
        "9:\n\t"
        "buffer_load_dwordx4 v239, %[srdws], %[t0] offen sc1 lds\n\t"
        "s_add_u32 m0, m0, 4096\n\t"
        "s_add_u32 %[t0], %[t0], 4096\n\t"
        "s_add_u32 %[t1], %[t1], 4096\n\t"
        "s_cmp_lt_u32 %[t1], 67584\n\t"
        "s_cbranch_scc1 9b\n\t"
        "s_add_u32 %[t0], %[skv], 173056\n\t"
        "s_add_u32 m0, %[hw1k], 108544\n\t"
        "s_mov_b32 %[t1], %[hw1k]\n\t"
        "9:\n\t"
        "buffer_load_dwordx4 v239, %[srdws], %[t0] offen sc1 lds\n\t"
        "s_add_u32 m0, m0, 4096\n\t"
        "s_add_u32 %[t0], %[t0], 4096\n\t"
        "s_add_u32 %[t1], %[t1], 4096\n\t"
        "s_cmp_lt_u32 %[t1], 7168\n\t"
        "s_cbranch_scc1 9b\n\t"
        "s_waitcnt vmcnt(0)\n\t"
        "s_mov_b64 exec, 1\n\t"
        "ds_add_u32 v230, v231\n\t"
        "s_mov_b64 exec, -1\n\t"
        "s_waitcnt lgkmcnt(0)\n\t"
        : "=&{s[68:69]}"(tag), [c] "+s"(c), [it] "=&s"(it), [t0] "=&s"(t0), [t1] "=&s"(t1), [p0] "=&s"(p0), "+s"(w2), "+s"(bid2),
          "=&{v[230:231]}"(in0), "+{v[232:239]}"(in), "=&{v[240:247]}"(in2), "=&{v[248:255]}"(in3)
        : [srdx] "s"(srdx), [srdq] "s"(srdq), [srdws] "s"(srdws), [hw1k] "s"(hw1k), [hw3k] "s"(hw3k), [skv] "s"(skv), [wsp] "s"(ws),
          "s"((unsigned)(size_t)lds)
        : "memory", "scc", "vcc", "v224", "v225", "v226", "v227");
    }
    const unsigned w = __builtin_amdgcn_readfirstlane(w2), bid = __builtin_amdgcn_readfirstlane(bid2), tb = ((bid >> 1) & 3) * 4 + (bid >> 6), b = bid & 1, hg = (bid >> 3) & 7, wid = tb * 16 + b * 8 + hg;
    const unsigned P = w & 1, Kh = w >> 1;
    const unsigned job = w == 0 ? 0 : ((w == 1) & (wid < 64)) ? 1 : 2, jj = wid & 7, ju = wid >> 3, mwt = L_WT + w * 5120;
    const rsrc_t srdo = mk((job ? ju >> 2 : ju >> 4) ? KARG(ah[0], ah[1]) : KARG((unsigned)aoA, (unsigned)(aoA >> 32)), 640 * 1280);
    const unsigned hw1k = w * 1024, skv = WS_KV + b * KV_BR + hw1k, first = bid | w;
    const unsigned ostep = job ? 40960 : 20480, dtr = job ? 256 : 512, dts = job ? 1280 : 2560;
    const unsigned sdst = sel(job, WS_W2T + (jj * 8 + ju) * 5120, WS_W2H + ((jj * 2 + (ju & 1)) * 16 + (ju >> 1)) * 5120);
    const unsigned sfw = WS_W2F + (jj * 64 + (job ? 32 : 0) + ju) * 64;
    const unsigned st4 = WS_W2T + hg * 40960, sw2 = WS_W2H + ((hg * 2 + P) * 16 + Kh) * 5120;
    const unsigned xrot = (hg & 3) * 20480, mox = P * 10240 + Kh * 5120, so = WS_OF + (tb * 4 + 2 * b + P) * 81920 + Kh * 5120;
    asm volatile(
      "s_setprio 3\n\t"
      "v_mov_b32 v228, s68\n\t"
      "v_mov_b32 v229, s69\n\t"
      "s_setprio 0\n\t"
      "s_cmp_lg_u32 %[first], 0\n\t"
      "s_cbranch_scc1 40f\n\t"
      "s_setprio 3\n\t"
      "v_xor_b32 v232, 0x2000, v232\n\t"
      "s_setprio 0\n\t"
      "s_mov_b32 %[it], 65536\n\t"
      "41:\n\t"
      "buffer_load_dwordx2 v[224:225], v232, %[srdws], 0 offen sc1\n\t"
      "buffer_load_dwordx2 v[226:227], v232, %[srdws], 0 offen offset:64 sc1\n\t"
      "s_waitcnt vmcnt(0)\n\t"
      "s_setprio 3\n\t"
      "v_cmp_ne_u64 vcc, s[68:69], v[224:225]\n\t"
      "v_cmp_ne_u64 %[p0], s[68:69], v[226:227]\n\t"
      "s_setprio 0\n\t"
      "s_or_b64 vcc, vcc, %[p0]\n\t"
      "s_cbranch_scc0 42f\n\t"
      "s_sleep 8\n\t"
      "s_sub_u32 %[it], %[it], 1\n\t"
      "s_cmp_lg_u32 %[it], 0\n\t"
      "s_cbranch_scc1 41b\n\t"
      "42:\n\t"
      "s_mov_b64 exec, 1\n\t"
      "buffer_store_dwordx2 v[228:229], off, %[srdws], 0 sc1\n\t"
      "s_mov_b64 exec, -1\n\t"
      "40:\n\t"
      "s_cmp_ge_u32 %[job], 2\n\t"
      "s_cbranch_scc1 50f\n\t"
      "s_mov_b32 m0, %[mwt]\n\t"
      "s_mov_b32 %[t0], 0\n\t"
      "s_waitcnt vmcnt(3)\n\t"
      "buffer_load_dwordx4 v240, %[srdo], %[t0] offen lds\n\t"
      "s_add_u32 m0, m0, 1024\n\t"
      "s_add_u32 %[t0], %[t0], %[ostep]\n\t"
      "s_waitcnt vmcnt(3)\n\t"
      "buffer_load_dwordx4 v240, %[srdo], %[t0] offen lds\n\t"
      "s_add_u32 m0, m0, 1024\n\t"
      "s_add_u32 %[t0], %[t0], %[ostep]\n\t"
      "s_waitcnt vmcnt(3)\n\t"
      "buffer_load_dwordx4 v240, %[srdo], %[t0] offen lds\n\t"
      "s_add_u32 m0, m0, 1024\n\t"
      "s_add_u32 %[t0], %[t0], %[ostep]\n\t"
      "s_waitcnt vmcnt(3)\n\t"
      "buffer_load_dwordx4 v240, %[srdo], %[t0] offen lds\n\t"
      "s_add_u32 m0, m0, 1024\n\t"
      "s_add_u32 %[t0], %[t0], %[ostep]\n\t"
      "s_waitcnt vmcnt(3)\n\t"
      "buffer_load_dwordx4 v240, %[srdo], %[t0] offen lds\n\t"
      "s_waitcnt vmcnt(0)\n\t"
      "ds_read_b64_tr_b16 v[0:1], v242 offset:0\n\t"
      "ds_read_b64_tr_b16 v[4:5], v242 offset:1280\n\t"
      "ds_read_b64_tr_b16 v[8:9], v242 offset:2560\n\t"
      "ds_read_b64_tr_b16 v[12:13], v242 offset:3840\n\t"
      "ds_read_b64_tr_b16 v[16:17], v243 offset:0\n\t"
      "s_setprio 3\n\t"
      "v_add_u32 v242, %[dtr], v242\n\t"
      "v_add_u32 v243, %[dts], v243\n\t"
      "s_setprio 0\n\t"
      "ds_read_b64_tr_b16 v[2:3], v242 offset:0\n\t"
      "ds_read_b64_tr_b16 v[6:7], v242 offset:1280\n\t"
      "ds_read_b64_tr_b16 v[10:11], v242 offset:2560\n\t"
      "ds_read_b64_tr_b16 v[14:15], v242 offset:3840\n\t"
      "ds_read_b64_tr_b16 v[18:19], v243 offset:0\n\t"
      "s_add_u32 %[t0], %[sdst], 4096\n\t"
      "s_waitcnt lgkmcnt(0)\n\t"
      "buffer_store_dwordx4 v[0:3], v239, %[srdws], %[sdst] offen offset:0 sc1\n\t"
      "buffer_store_dwordx4 v[4:7], v239, %[srdws], %[sdst] offen offset:1024 sc1\n\t"
      "buffer_store_dwordx4 v[8:11], v239, %[srdws], %[sdst] offen offset:2048 sc1\n\t"
      "buffer_store_dwordx4 v[12:15], v239, %[srdws], %[sdst] offen offset:3072 sc1\n\t"
      "buffer_store_dwordx4 v[16:19], v239, %[srdws], %[t0] offen offset:0 sc1\n\t"
      "s_waitcnt vmcnt(0)\n\t"
      "s_mov_b64 exec, 1\n\t"
      "buffer_store_dwordx2 v[228:229], off, %[srdws], %[sfw] sc1\n\t"
      "s_mov_b64 exec, -1\n\t"
      "50:\n\t"
      "s_sleep 64\n\t"
      "s_bfm_b64 %[p0], 40, 0\n\t"
      "s_mov_b32 %[it], 65536\n\t"
      "52:\n\t"
      "buffer_load_dwordx2 v[224:225], v249, %[srdws], 0 offen sc1\n\t"
      "s_waitcnt vmcnt(0)\n\t"
      "s_setprio 3\n\t"
      "v_cmp_ne_u64 vcc, s[68:69], v[224:225]\n\t"
      "s_setprio 0\n\t"
      "s_and_b64 vcc, vcc, %[p0]\n\t"
      "s_cbranch_scc0 53f\n\t"
      "s_sleep 8\n\t"
      "s_sub_u32 %[it], %[it], 1\n\t"
      "s_cmp_lg_u32 %[it], 0\n\t"
      "s_cbranch_scc1 52b\n\t"
      "53:\n\t"
      "s_mov_b32 %[t1], %[hw1k]\n\t"
      "54:\n\t"
      "s_add_u32 m0, %[t1], 0\n\t"
      "s_add_u32 %[t0], %[t1], %[st4]\n\t"
      "s_waitcnt vmcnt(3)\n\t"
      "buffer_load_dwordx4 v239, %[srdws], %[t0] offen sc1 lds\n\t"
      "s_add_u32 %[t1], %[t1], 4096\n\t"
      "s_cmp_lt_u32 %[t1], 40960\n\t"
      "s_cbranch_scc1 54b\n\t"
      "s_mov_b32 %[it], 1048576\n\t"
      "62:\n\t"
      "ds_read_b32 v224, v255 offset:16\n\t"
      "s_waitcnt lgkmcnt(0)\n\t"
      "s_setprio 3\n\t"
      "v_cmp_gt_u32 vcc, 1, v224\n\t"
      "s_setprio 0\n\t"
      "s_cmp_eq_u64 vcc, 0\n\t"
      "s_cbranch_scc1 63f\n\t"
      "s_sleep 8\n\t"
      "s_sub_u32 %[it], %[it], 1\n\t"
      "s_cmp_lg_u32 %[it], 0\n\t"
      "s_cbranch_scc1 62b\n\t"
      "63:\n\t"
      "s_add_u32 %[t0], %[skv], 105472\n\t"
      "s_add_u32 m0, %[hw1k], 40960\n\t"
      "s_mov_b32 %[t1], %[hw1k]\n\t"
      "9:\n\t"
      "s_waitcnt vmcnt(3)\n\t"
      "buffer_load_dwordx4 v239, %[srdws], %[t0] offen sc1 lds\n\t"
      "s_add_u32 m0, m0, 4096\n\t"
      "s_add_u32 %[t0], %[t0], 4096\n\t"
      "s_add_u32 %[t1], %[t1], 4096\n\t"
      "s_cmp_lt_u32 %[t1], 33792\n\t"
      "s_cbranch_scc1 9b\n\t"
      "s_waitcnt vmcnt(0)\n\t"
      "s_mov_b64 exec, 1\n\t"
      "ds_add_u32 v230, v231\n\t"
      "s_mov_b64 exec, -1\n\t"
      "s_sleep 80\n\t"
      "s_mov_b32 %[it], 1048576\n\t"
      "62:\n\t"
      "ds_read_b32 v224, v255 offset:16\n\t"
      "s_waitcnt lgkmcnt(0)\n\t"
      "s_setprio 3\n\t"
      "v_cmp_gt_u32 vcc, 2, v224\n\t"
      "s_setprio 0\n\t"
      "s_cmp_eq_u64 vcc, 0\n\t"
      "s_cbranch_scc1 63f\n\t"
      "s_sleep 8\n\t"
      "s_sub_u32 %[it], %[it], 1\n\t"
      "s_cmp_lg_u32 %[it], 0\n\t"
      "s_cbranch_scc1 62b\n\t"
      "63:\n\t"
      "s_add_u32 %[t0], %[skv], 139264\n\t"
      "s_add_u32 m0, %[hw1k], 74752\n\t"
      "s_mov_b32 %[t1], %[hw1k]\n\t"
      "9:\n\t"
      "s_waitcnt vmcnt(3)\n\t"
      "buffer_load_dwordx4 v239, %[srdws], %[t0] offen sc1 lds\n\t"
      "s_add_u32 m0, m0, 4096\n\t"
      "s_add_u32 %[t0], %[t0], 4096\n\t"
      "s_add_u32 %[t1], %[t1], 4096\n\t"
      "s_cmp_lt_u32 %[t1], 33792\n\t"
      "s_cbranch_scc1 9b\n\t"
      "s_waitcnt vmcnt(0)\n\t"
      "s_mov_b64 exec, 1\n\t"
      "ds_add_u32 v230, v231\n\t"
      "s_mov_b64 exec, -1\n\t"
      "s_add_u32 %[t0], %[sw2], 0\n\t"
      "s_add_u32 %[t1], %[t0], 4096\n\t"
      "s_waitcnt vmcnt(3)\n\t"
      "buffer_load_dwordx4 v[0:3], v239, %[srdws], %[t0] offen offset:0 sc1\n\t"
      "s_waitcnt vmcnt(3)\n\t"
      "buffer_load_dwordx4 v[4:7], v239, %[srdws], %[t0] offen offset:1024 sc1\n\t"
      "s_waitcnt vmcnt(3)\n\t"
      "buffer_load_dwordx4 v[8:11], v239, %[srdws], %[t0] offen offset:2048 sc1\n\t"
      "s_waitcnt vmcnt(3)\n\t"
      "buffer_load_dwordx4 v[12:15], v239, %[srdws], %[t0] offen offset:3072 sc1\n\t"
      "s_waitcnt vmcnt(3)\n\t"
      "buffer_load_dwordx2 v[16:17], v239, %[srdws], %[t1] offen offset:0 sc1\n\t"
      "s_waitcnt vmcnt(3)\n\t"
      "buffer_load_dwordx2 v[36:37], v239, %[srdws], %[t1] offen offset:8 sc1\n\t"
      "s_add_u32 %[t0], %[sw2], 10240\n\t"
      "s_add_u32 %[t1], %[t0], 4096\n\t"
      "s_waitcnt vmcnt(3)\n\t"
      "buffer_load_dwordx4 v[20:23], v239, %[srdws], %[t0] offen offset:0 sc1\n\t"
      "s_waitcnt vmcnt(3)\n\t"
      "buffer_load_dwordx4 v[24:27], v239, %[srdws], %[t0] offen offset:1024 sc1\n\t"
      "s_waitcnt vmcnt(3)\n\t"
      "buffer_load_dwordx4 v[28:31], v239, %[srdws], %[t0] offen offset:2048 sc1\n\t"
      "s_waitcnt vmcnt(3)\n\t"
      "buffer_load_dwordx4 v[32:35], v239, %[srdws], %[t0] offen offset:3072 sc1\n\t"
      "s_waitcnt vmcnt(3)\n\t"
      "buffer_load_dwordx2 v[18:19], v239, %[srdws], %[t1] offen offset:0 sc1\n\t"
      "s_waitcnt vmcnt(3)\n\t"
      "buffer_load_dwordx2 v[38:39], v239, %[srdws], %[t1] offen offset:8 sc1\n\t"
      "s_add_u32 %[t0], %[sw2], 20480\n\t"
      "s_add_u32 %[t1], %[t0], 4096\n\t"
      "s_waitcnt vmcnt(3)\n\t"
      "buffer_load_dwordx4 v[40:43], v239, %[srdws], %[t0] offen offset:0 sc1\n\t"
      "s_waitcnt vmcnt(3)\n\t"
      "buffer_load_dwordx4 v[44:47], v239, %[srdws], %[t0] offen offset:1024 sc1\n\t"
      "s_waitcnt vmcnt(3)\n\t"
      "buffer_load_dwordx4 v[48:51], v239, %[srdws], %[t0] offen offset:2048 sc1\n\t"
      "s_waitcnt vmcnt(3)\n\t"
      "buffer_load_dwordx4 v[52:55], v239, %[srdws], %[t0] offen offset:3072 sc1\n\t"
      "s_waitcnt vmcnt(3)\n\t"
      "buffer_load_dwordx2 v[56:57], v239, %[srdws], %[t1] offen offset:0 sc1\n\t"
      "s_waitcnt vmcnt(3)\n\t"
      "buffer_load_dwordx2 v[76:77], v239, %[srdws], %[t1] offen offset:8 sc1\n\t"
      "s_add_u32 %[t0], %[sw2], 30720\n\t"
      "s_add_u32 %[t1], %[t0], 4096\n\t"
      "s_waitcnt vmcnt(3)\n\t"
      "buffer_load_dwordx4 v[60:63], v239, %[srdws], %[t0] offen offset:0 sc1\n\t"
      "s_waitcnt vmcnt(3)\n\t"
      "buffer_load_dwordx4 v[64:67], v239, %[srdws], %[t0] offen offset:1024 sc1\n\t"
      "s_waitcnt vmcnt(3)\n\t"
      "buffer_load_dwordx4 v[68:71], v239, %[srdws], %[t0] offen offset:2048 sc1\n\t"
      "s_waitcnt vmcnt(3)\n\t"
      "buffer_load_dwordx4 v[72:75], v239, %[srdws], %[t0] offen offset:3072 sc1\n\t"
      "s_waitcnt vmcnt(3)\n\t"
      "buffer_load_dwordx2 v[58:59], v239, %[srdws], %[t1] offen offset:0 sc1\n\t"
      "s_waitcnt vmcnt(3)\n\t"
      "buffer_load_dwordx2 v[78:79], v239, %[srdws], %[t1] offen offset:8 sc1\n\t"
      "s_add_u32 %[t0], %[sw2], 40960\n\t"
      "s_add_u32 %[t1], %[t0], 4096\n\t"
      "s_waitcnt vmcnt(3)\n\t"
      "buffer_load_dwordx4 v[80:83], v239, %[srdws], %[t0] offen offset:0 sc1\n\t"
      "s_waitcnt vmcnt(3)\n\t"
      "buffer_load_dwordx4 v[84:87], v239, %[srdws], %[t0] offen offset:1024 sc1\n\t"
      "s_waitcnt vmcnt(3)\n\t"
      "buffer_load_dwordx4 v[88:91], v239, %[srdws], %[t0] offen offset:2048 sc1\n\t"
      "s_waitcnt vmcnt(3)\n\t"
      "buffer_load_dwordx4 v[92:95], v239, %[srdws], %[t0] offen offset:3072 sc1\n\t"
      "s_waitcnt vmcnt(3)\n\t"
      "buffer_load_dwordx2 v[96:97], v239, %[srdws], %[t1] offen offset:0 sc1\n\t"
      "s_waitcnt vmcnt(3)\n\t"
      "buffer_load_dwordx2 v[116:117], v239, %[srdws], %[t1] offen offset:8 sc1\n\t"
      "s_add_u32 %[t0], %[sw2], 51200\n\t"
      "s_add_u32 %[t1], %[t0], 4096\n\t"
      "s_waitcnt vmcnt(3)\n\t"
      "buffer_load_dwordx4 v[100:103], v239, %[srdws], %[t0] offen offset:0 sc1\n\t"
      "s_waitcnt vmcnt(3)\n\t"
      "buffer_load_dwordx4 v[104:107], v239, %[srdws], %[t0] offen offset:1024 sc1\n\t"
      "s_waitcnt vmcnt(3)\n\t"
      "buffer_load_dwordx4 v[108:111], v239, %[srdws], %[t0] offen offset:2048 sc1\n\t"
      "s_waitcnt vmcnt(3)\n\t"
      "buffer_load_dwordx4 v[112:115], v239, %[srdws], %[t0] offen offset:3072 sc1\n\t"
      "s_waitcnt vmcnt(3)\n\t"
      "buffer_load_dwordx2 v[98:99], v239, %[srdws], %[t1] offen offset:0 sc1\n\t"
      "s_waitcnt vmcnt(3)\n\t"
      "buffer_load_dwordx2 v[118:119], v239, %[srdws], %[t1] offen offset:8 sc1\n\t"
      "s_add_u32 %[t0], %[sw2], 61440\n\t"
      "s_add_u32 %[t1], %[t0], 4096\n\t"
      "s_waitcnt vmcnt(3)\n\t"
      "buffer_load_dwordx4 v[120:123], v239, %[srdws], %[t0] offen offset:0 sc1\n\t"
      "s_waitcnt vmcnt(3)\n\t"
      "buffer_load_dwordx4 v[124:127], v239, %[srdws], %[t0] offen offset:1024 sc1\n\t"
      "s_waitcnt vmcnt(3)\n\t"
      "buffer_load_dwordx4 v[128:131], v239, %[srdws], %[t0] offen offset:2048 sc1\n\t"
      "s_waitcnt vmcnt(3)\n\t"
      "buffer_load_dwordx4 v[132:135], v239, %[srdws], %[t0] offen offset:3072 sc1\n\t"
      "s_waitcnt vmcnt(3)\n\t"
      "buffer_load_dwordx2 v[136:137], v239, %[srdws], %[t1] offen offset:0 sc1\n\t"
      "s_waitcnt vmcnt(3)\n\t"
      "buffer_load_dwordx2 v[156:157], v239, %[srdws], %[t1] offen offset:8 sc1\n\t"
      "s_add_u32 %[t0], %[sw2], 71680\n\t"
      "s_add_u32 %[t1], %[t0], 4096\n\t"
      "s_waitcnt vmcnt(3)\n\t"
      "buffer_load_dwordx4 v[140:143], v239, %[srdws], %[t0] offen offset:0 sc1\n\t"
      "s_waitcnt vmcnt(3)\n\t"
      "buffer_load_dwordx4 v[144:147], v239, %[srdws], %[t0] offen offset:1024 sc1\n\t"
      "s_waitcnt vmcnt(3)\n\t"
      "buffer_load_dwordx4 v[148:151], v239, %[srdws], %[t0] offen offset:2048 sc1\n\t"
      "s_waitcnt vmcnt(3)\n\t"
      "buffer_load_dwordx4 v[152:155], v239, %[srdws], %[t0] offen offset:3072 sc1\n\t"
      "s_waitcnt vmcnt(3)\n\t"
      "buffer_load_dwordx2 v[138:139], v239, %[srdws], %[t1] offen offset:0 sc1\n\t"
      "s_waitcnt vmcnt(3)\n\t"
      "buffer_load_dwordx2 v[158:159], v239, %[srdws], %[t1] offen offset:8 sc1\n\t"
      "s_setprio 3\n\t"
      "v_add_u32 v224, %[hw1k], v239\n\t"
      "v_lshlrev_b32 v224, 3, v224\n\t"
      "s_setprio 0\n\t"
      "s_sleep 127\n\t"
      "buffer_load_dword v226, v224, %[srdc], 0 offen\n\t"
      "s_sleep 58\n\t"
      "s_mov_b32 %[it], 1048576\n\t"
      "64:\n\t"
      "ds_read_b32 v224, v255 offset:16\n\t"
      "s_waitcnt lgkmcnt(0)\n\t"
      "s_setprio 3\n\t"
      "v_cmp_gt_u32 vcc, 4, v224\n\t"
      "s_setprio 0\n\t"
      "s_cmp_eq_u64 vcc, 0\n\t"
      "s_cbranch_scc1 65f\n\t"
      "s_sleep 4\n\t"
      "s_sub_u32 %[it], %[it], 1\n\t"
      "s_cmp_lg_u32 %[it], 0\n\t"
      "s_cbranch_scc1 64b\n\t"
      "65:\n\t"
      "s_sleep 8\n\t"
      "s_mov_b64 %[p0], 0xff\n\t"
      "s_mov_b32 %[it], 65536\n\t"
      "56:\n\t"
      "buffer_load_dwordx2 v[224:225], v241, %[srdws], 0 offen sc1\n\t"
      "s_waitcnt vmcnt(0)\n\t"
      "s_setprio 3\n\t"
      "v_cmp_ne_u64 vcc, s[68:69], v[224:225]\n\t"
      "s_setprio 0\n\t"
      "s_and_b64 vcc, vcc, %[p0]\n\t"
      "s_cbranch_scc0 57f\n\t"
      "s_sleep 8\n\t"
      "s_sub_u32 %[it], %[it], 1\n\t"
      "s_cmp_lg_u32 %[it], 0\n\t"
      "s_cbranch_scc1 56b\n\t"
      "57:\n\t"
      "s_add_u32 %[c], %[xrot], %[so]\n\t"
      "s_add_u32 %[t1], %[xrot], %[mox]\n\t"
      "s_add_u32 %[it], %[so], 81920\n\t"
      "s_add_u32 m0, %[t1], 40960\n\t"
      "s_add_u32 %[t0], %[c], 8192\n\t"
      "s_waitcnt vmcnt(6)\n\t"
      "buffer_load_dwordx4 v239, %[srdws], %[c] offen sc1 lds\n\t"
      "s_waitcnt vmcnt(6)\n\t"
      "buffer_load_dwordx4 v239, %[srdws], %[c] offen offset:1024 sc1 lds\n\t"
      "s_waitcnt vmcnt(6)\n\t"
      "buffer_load_dwordx4 v239, %[srdws], %[t0] offen offset:2048 sc1 lds\n\t"
      "s_waitcnt vmcnt(6)\n\t"
      "buffer_load_dwordx4 v239, %[srdws], %[t0] offen offset:3072 sc1 lds\n\t"
      "s_add_u32 m0, %[t1], 45056\n\t"
      "s_nop 0\n\t"
      "s_waitcnt vmcnt(6)\n\t"
      "buffer_load_dwordx4 v252, %[srdws], %[c] offen sc1 lds\n\t"
      "s_add_u32 %[c], %[c], 20480\n\t"
      "s_cmp_eq_u32 %[c], %[it]\n\t"
      "s_cselect_b32 %[c], %[so], %[c]\n\t"
      "s_sub_u32 %[t1], %[c], %[so]\n\t"
      "s_add_u32 %[t1], %[t1], %[mox]\n\t"
      "s_add_u32 m0, %[t1], 40960\n\t"
      "s_add_u32 %[t0], %[c], 8192\n\t"
      "s_waitcnt vmcnt(6)\n\t"
      "buffer_load_dwordx4 v239, %[srdws], %[c] offen sc1 lds\n\t"
      "s_waitcnt vmcnt(6)\n\t"
      "buffer_load_dwordx4 v239, %[srdws], %[c] offen offset:1024 sc1 lds\n\t"
      "s_waitcnt vmcnt(6)\n\t"
      "buffer_load_dwordx4 v239, %[srdws], %[t0] offen offset:2048 sc1 lds\n\t"
      "s_waitcnt vmcnt(6)\n\t"
      "buffer_load_dwordx4 v239, %[srdws], %[t0] offen offset:3072 sc1 lds\n\t"
      "s_add_u32 m0, %[t1], 45056\n\t"
      "s_nop 0\n\t"
      "s_waitcnt vmcnt(6)\n\t"
      "buffer_load_dwordx4 v252, %[srdws], %[c] offen sc1 lds\n\t"
      "s_add_u32 %[c], %[c], 20480\n\t"
      "s_cmp_eq_u32 %[c], %[it]\n\t"
      "s_cselect_b32 %[c], %[so], %[c]\n\t"
      "s_sub_u32 %[t1], %[c], %[so]\n\t"
      "s_add_u32 %[t1], %[t1], %[mox]\n\t"
      "s_add_u32 m0, %[t1], 40960\n\t"
      "s_add_u32 %[t0], %[c], 8192\n\t"
      "s_waitcnt vmcnt(6)\n\t"
      "buffer_load_dwordx4 v239, %[srdws], %[c] offen sc1 lds\n\t"
      "s_waitcnt vmcnt(6)\n\t"
      "buffer_load_dwordx4 v239, %[srdws], %[c] offen offset:1024 sc1 lds\n\t"
      "s_waitcnt vmcnt(6)\n\t"
      "buffer_load_dwordx4 v239, %[srdws], %[t0] offen offset:2048 sc1 lds\n\t"
      "s_waitcnt vmcnt(6)\n\t"
      "buffer_load_dwordx4 v239, %[srdws], %[t0] offen offset:3072 sc1 lds\n\t"
      "s_add_u32 m0, %[t1], 45056\n\t"
      "s_nop 0\n\t"
      "s_waitcnt vmcnt(6)\n\t"
      "buffer_load_dwordx4 v252, %[srdws], %[c] offen sc1 lds\n\t"
      "s_add_u32 %[c], %[c], 20480\n\t"
      "s_cmp_eq_u32 %[c], %[it]\n\t"
      "s_cselect_b32 %[c], %[so], %[c]\n\t"
      "s_sub_u32 %[t1], %[c], %[so]\n\t"
      "s_add_u32 %[t1], %[t1], %[mox]\n\t"
      "s_add_u32 m0, %[t1], 40960\n\t"
      "s_add_u32 %[t0], %[c], 8192\n\t"
      "s_waitcnt vmcnt(6)\n\t"
      "buffer_load_dwordx4 v239, %[srdws], %[c] offen sc1 lds\n\t"
      "s_waitcnt vmcnt(6)\n\t"
      "buffer_load_dwordx4 v239, %[srdws], %[c] offen offset:1024 sc1 lds\n\t"
      "s_waitcnt vmcnt(6)\n\t"
      "buffer_load_dwordx4 v239, %[srdws], %[t0] offen offset:2048 sc1 lds\n\t"
      "s_waitcnt vmcnt(6)\n\t"
      "buffer_load_dwordx4 v239, %[srdws], %[t0] offen offset:3072 sc1 lds\n\t"
      "s_add_u32 m0, %[t1], 45056\n\t"
      "s_nop 0\n\t"
      "s_waitcnt vmcnt(6)\n\t"
      "buffer_load_dwordx4 v252, %[srdws], %[c] offen sc1 lds\n\t"
      "s_add_u32 %[c], %[c], 20480\n\t"
      "s_cmp_eq_u32 %[c], %[it]\n\t"
      "s_cselect_b32 %[c], %[so], %[c]\n\t"
      "s_sub_u32 %[t1], %[c], %[so]\n\t"
      "s_add_u32 %[t1], %[t1], %[mox]\n\t"
      "s_waitcnt vmcnt(0)\n\t"
      "s_mov_b64 exec, 1\n\t"
      "ds_add_u32 v230, v231\n\t"
      "ds_add_u32 v230, v231\n\t"
      "s_mov_b64 exec, -1\n\t"
      "s_mov_b32 %[it], 1048576\n\t"
      "58:\n\t"
      "ds_read_b32 v226, v255 offset:0\n\t"
      "s_waitcnt lgkmcnt(0)\n\t"
      "s_setprio 3\n\t"
      "v_cmp_gt_u32 vcc, 6, v226\n\t"
      "s_setprio 0\n\t"
      "s_cmp_eq_u64 vcc, 0\n\t"
      "s_cbranch_scc1 59f\n\t"
      "s_sleep 2\n\t"
      "s_sub_u32 %[it], %[it], 1\n\t"
      "s_cmp_lg_u32 %[it], 0\n\t"
      "s_cbranch_scc1 58b\n\t"
      "59:\n\t"
      "s_mov_b32 %[it], 1048576\n\t"
      "60:\n\t"
      "ds_read_b32 v226, v255 offset:16\n\t"
      "s_waitcnt lgkmcnt(0)\n\t"
      "s_setprio 3\n\t"
      "v_cmp_gt_u32 vcc, 5, v226\n\t"
      "s_setprio 0\n\t"
      "s_cmp_eq_u64 vcc, 0\n\t"
      "s_cbranch_scc1 61f\n\t"
      "s_sleep 3\n\t"
      "s_sub_u32 %[it], %[it], 1\n\t"
      "s_cmp_lg_u32 %[it], 0\n\t"
      "s_cbranch_scc1 60b\n\t"
      "61:\n\t"
      "ds_read_b128 v[192:195], v244 offset:0\n\t"
      "ds_read_b128 v[196:199], v244 offset:1024\n\t"
      "ds_read_b128 v[200:203], v244 offset:2048\n\t"
      "ds_read_b128 v[204:207], v244 offset:3072\n\t"
      "ds_read2st64_b64 v[208:211], v253 offset0:8 offset1:9\n\t"
      "s_waitcnt lgkmcnt(4)\n\t"
      "v_mfma_f32_32x32x16_bf16 v[160:175], v[0:3], v[192:195], 0\n\t"
      "ds_read_b128 v[212:215], v246 offset:0\n\t"
      "s_waitcnt lgkmcnt(4)\n\t"
      "v_mfma_f32_32x32x16_bf16 v[160:175], v[4:7], v[196:199], v[160:175]\n\t"
      "ds_read_b128 v[216:219], v246 offset:1024\n\t"
      "s_waitcnt lgkmcnt(4)\n\t"
      "v_mfma_f32_32x32x16_bf16 v[160:175], v[20:23], v[200:203], v[160:175]\n\t"
      "ds_read_b128 v[220:223], v246 offset:2048\n\t"
      "s_waitcnt lgkmcnt(4)\n\t"
      "v_mfma_f32_32x32x16_bf16 v[160:175], v[24:27], v[204:207], v[160:175]\n\t"
      "ds_read_b128 v[192:195], v246 offset:3072\n\t"
      "s_waitcnt lgkmcnt(4)\n\t"
      "v_mfma_f32_32x32x16_bf16 v[160:175], v[16:19], v[208:211], v[160:175]\n\t"
      "ds_read2st64_b64 v[196:199], v254 offset0:8 offset1:9\n\t"
      "s_waitcnt lgkmcnt(4)\n\t"
      "v_mfma_f32_32x32x16_bf16 v[176:191], v[0:3], v[212:215], 0\n\t"
      "ds_read_b128 v[200:203], v244 offset:20480\n\t"
      "s_waitcnt lgkmcnt(4)\n\t"
      "v_mfma_f32_32x32x16_bf16 v[176:191], v[4:7], v[216:219], v[176:191]\n\t"
      "ds_read_b128 v[204:207], v244 offset:21504\n\t"
      "s_waitcnt lgkmcnt(4)\n\t"
      "v_mfma_f32_32x32x16_bf16 v[176:191], v[20:23], v[220:223], v[176:191]\n\t"
      "ds_read_b128 v[208:211], v244 offset:22528\n\t"
      "s_waitcnt lgkmcnt(4)\n\t"
      "v_mfma_f32_32x32x16_bf16 v[176:191], v[24:27], v[192:195], v[176:191]\n\t"
      "ds_read_b128 v[212:215], v244 offset:23552\n\t"
      "s_waitcnt lgkmcnt(4)\n\t"
      "v_mfma_f32_32x32x16_bf16 v[176:191], v[16:19], v[196:199], v[176:191]\n\t"
      "ds_read2st64_b64 v[216:219], v253 offset0:48 offset1:49\n\t"
      "s_waitcnt lgkmcnt(4)\n\t"
      "v_mfma_f32_32x32x16_bf16 v[160:175], v[40:43], v[200:203], v[160:175]\n\t"
      "ds_read_b128 v[220:223], v246 offset:20480\n\t"
      "s_waitcnt lgkmcnt(4)\n\t"
      "v_mfma_f32_32x32x16_bf16 v[160:175], v[44:47], v[204:207], v[160:175]\n\t"
      "ds_read_b128 v[192:195], v246 offset:21504\n\t"
      "s_waitcnt lgkmcnt(4)\n\t"
      "v_mfma_f32_32x32x16_bf16 v[160:175], v[60:63], v[208:211], v[160:175]\n\t"
      "ds_read_b128 v[196:199], v246 offset:22528\n\t"
      "s_waitcnt lgkmcnt(4)\n\t"
      "v_mfma_f32_32x32x16_bf16 v[160:175], v[64:67], v[212:215], v[160:175]\n\t"
      "ds_read_b128 v[200:203], v246 offset:23552\n\t"
      "s_waitcnt lgkmcnt(4)\n\t"
      "v_mfma_f32_32x32x16_bf16 v[160:175], v[56:59], v[216:219], v[160:175]\n\t"
      "ds_read2st64_b64 v[204:207], v254 offset0:48 offset1:49\n\t"
      "s_waitcnt lgkmcnt(4)\n\t"
      "v_mfma_f32_32x32x16_bf16 v[176:191], v[40:43], v[220:223], v[176:191]\n\t"
      "v_add_u32 v244, 0xa000, v244\n\t"
      "v_add_u32 v246, 0xa000, v246\n\t"
      "v_add_u32 v253, 0xa000, v253\n\t"
      "v_add_u32 v254, 0xa000, v254\n\t"
      "ds_read_b128 v[208:211], v244 offset:0\n\t"
      "s_waitcnt lgkmcnt(4)\n\t"
      "v_mfma_f32_32x32x16_bf16 v[176:191], v[44:47], v[192:195], v[176:191]\n\t"
      "ds_read_b128 v[212:215], v244 offset:1024\n\t"
      "s_waitcnt lgkmcnt(4)\n\t"
      "v_mfma_f32_32x32x16_bf16 v[176:191], v[60:63], v[196:199], v[176:191]\n\t"
      "ds_read_b128 v[216:219], v244 offset:2048\n\t"
      "s_waitcnt lgkmcnt(4)\n\t"
      "v_mfma_f32_32x32x16_bf16 v[176:191], v[64:67], v[200:203], v[176:191]\n\t"
      "ds_read_b128 v[220:223], v244 offset:3072\n\t"
      "s_waitcnt lgkmcnt(4)\n\t"
      "v_mfma_f32_32x32x16_bf16 v[176:191], v[56:59], v[204:207], v[176:191]\n\t"
      "ds_read2st64_b64 v[192:195], v253 offset0:8 offset1:9\n\t"
      "s_waitcnt lgkmcnt(4)\n\t"
      "v_mfma_f32_32x32x16_bf16 v[160:175], v[80:83], v[208:211], v[160:175]\n\t"
      "ds_read_b128 v[196:199], v246 offset:0\n\t"
      "s_waitcnt lgkmcnt(4)\n\t"
      "v_mfma_f32_32x32x16_bf16 v[160:175], v[84:87], v[212:215], v[160:175]\n\t"
      "ds_read_b128 v[200:203], v246 offset:1024\n\t"
      "s_waitcnt lgkmcnt(4)\n\t"
      "v_mfma_f32_32x32x16_bf16 v[160:175], v[100:103], v[216:219], v[160:175]\n\t"
      "ds_read_b128 v[204:207], v246 offset:2048\n\t"
      "s_waitcnt lgkmcnt(4)\n\t"
      "v_mfma_f32_32x32x16_bf16 v[160:175], v[104:107], v[220:223], v[160:175]\n\t"
      "ds_read_b128 v[208:211], v246 offset:3072\n\t"
      "s_waitcnt lgkmcnt(4)\n\t"
      "v_mfma_f32_32x32x16_bf16 v[160:175], v[96:99], v[192:195], v[160:175]\n\t"
      "ds_read2st64_b64 v[212:215], v254 offset0:8 offset1:9\n\t"
      "s_waitcnt lgkmcnt(4)\n\t"
      "v_mfma_f32_32x32x16_bf16 v[176:191], v[80:83], v[196:199], v[176:191]\n\t"
      "ds_read_b128 v[216:219], v244 offset:20480\n\t"
      "s_waitcnt lgkmcnt(4)\n\t"
      "v_mfma_f32_32x32x16_bf16 v[176:191], v[84:87], v[200:203], v[176:191]\n\t"
      "ds_read_b128 v[220:223], v244 offset:21504\n\t"
      "s_waitcnt lgkmcnt(4)\n\t"
      "v_mfma_f32_32x32x16_bf16 v[176:191], v[100:103], v[204:207], v[176:191]\n\t"
      "ds_read_b128 v[192:195], v244 offset:22528\n\t"
      "s_waitcnt lgkmcnt(4)\n\t"
      "v_mfma_f32_32x32x16_bf16 v[176:191], v[104:107], v[208:211], v[176:191]\n\t"
      "ds_read_b128 v[196:199], v244 offset:23552\n\t"
      "s_waitcnt lgkmcnt(4)\n\t"
      "v_mfma_f32_32x32x16_bf16 v[176:191], v[96:99], v[212:215], v[176:191]\n\t"
      "ds_read2st64_b64 v[200:203], v253 offset0:48 offset1:49\n\t"
      "s_waitcnt lgkmcnt(4)\n\t"
      "v_mfma_f32_32x32x16_bf16 v[160:175], v[120:123], v[216:219], v[160:175]\n\t"
      "ds_read_b128 v[204:207], v246 offset:20480\n\t"
      "s_waitcnt lgkmcnt(4)\n\t"
      "v_mfma_f32_32x32x16_bf16 v[160:175], v[124:127], v[220:223], v[160:175]\n\t"
      "ds_read_b128 v[208:211], v246 offset:21504\n\t"
      "s_waitcnt lgkmcnt(4)\n\t"
      "v_mfma_f32_32x32x16_bf16 v[160:175], v[140:143], v[192:195], v[160:175]\n\t"
      "ds_read_b128 v[212:215], v246 offset:22528\n\t"
      "s_waitcnt lgkmcnt(4)\n\t"
      "v_mfma_f32_32x32x16_bf16 v[160:175], v[144:147], v[196:199], v[160:175]\n\t"
      "ds_read_b128 v[216:219], v246 offset:23552\n\t"
      "s_waitcnt lgkmcnt(4)\n\t"
      "v_mfma_f32_32x32x16_bf16 v[160:175], v[136:139], v[200:203], v[160:175]\n\t"
      "ds_read2st64_b64 v[220:223], v254 offset0:48 offset1:49\n\t"
      "s_waitcnt lgkmcnt(4)\n\t"
      "v_mfma_f32_32x32x16_bf16 v[176:191], v[120:123], v[204:207], v[176:191]\n\t"
      "s_waitcnt lgkmcnt(3)\n\t"
      "v_mfma_f32_32x32x16_bf16 v[176:191], v[124:127], v[208:211], v[176:191]\n\t"
      "s_waitcnt lgkmcnt(2)\n\t"
      "v_mfma_f32_32x32x16_bf16 v[176:191], v[140:143], v[212:215], v[176:191]\n\t"
      "s_waitcnt lgkmcnt(1)\n\t"
      "v_mfma_f32_32x32x16_bf16 v[176:191], v[144:147], v[216:219], v[176:191]\n\t"
      "s_waitcnt lgkmcnt(0)\n\t"
      "v_mfma_f32_32x32x16_bf16 v[176:191], v[136:139], v[220:223], v[176:191]\n\t"
      "s_barrier\n\t"
      "v_add_u32 v244, 0xfffec000, v244\n\t"
      "v_add_u32 v246, 0xfffec000, v246\n\t"
      "v_add_u32 v253, 0xfffec000, v253\n\t"
      "v_add_u32 v254, 0xfffec000, v254\n\t"
      "ds_read_b128 v[192:195], v244 offset:0\n\t"
      "ds_read_b128 v[196:199], v244 offset:1024\n\t"
      "ds_read_b128 v[200:203], v244 offset:2048\n\t"
      "ds_read_b128 v[204:207], v244 offset:3072\n\t"
      "ds_read2st64_b64 v[208:211], v253 offset0:8 offset1:9\n\t"
      "s_waitcnt lgkmcnt(4)\n\t"
      "v_mfma_f32_32x32x16_bf16 v[160:175], v[8:11], v[192:195], v[160:175]\n\t"
      "ds_read_b128 v[212:215], v246 offset:0\n\t"
      "s_waitcnt lgkmcnt(4)\n\t"
      "v_mfma_f32_32x32x16_bf16 v[160:175], v[12:15], v[196:199], v[160:175]\n\t"
      "ds_read_b128 v[216:219], v246 offset:1024\n\t"
      "s_waitcnt lgkmcnt(4)\n\t"
      "v_mfma_f32_32x32x16_bf16 v[160:175], v[28:31], v[200:203], v[160:175]\n\t"
      "ds_read_b128 v[220:223], v246 offset:2048\n\t"
      "s_waitcnt lgkmcnt(4)\n\t"
      "v_mfma_f32_32x32x16_bf16 v[160:175], v[32:35], v[204:207], v[160:175]\n\t"
      "ds_read_b128 v[192:195], v246 offset:3072\n\t"
      "s_waitcnt lgkmcnt(4)\n\t"
      "v_mfma_f32_32x32x16_bf16 v[160:175], v[36:39], v[208:211], v[160:175]\n\t"
      "ds_read2st64_b64 v[196:199], v254 offset0:8 offset1:9\n\t"
      "s_waitcnt lgkmcnt(4)\n\t"
      "v_mfma_f32_32x32x16_bf16 v[176:191], v[8:11], v[212:215], v[176:191]\n\t"
      "s_barrier\n\t"
      "ds_read_b128 v[200:203], v244 offset:20480\n\t"
      "s_waitcnt lgkmcnt(4)\n\t"
      "v_mfma_f32_32x32x16_bf16 v[176:191], v[12:15], v[216:219], v[176:191]\n\t"
      "ds_read_b128 v[204:207], v244 offset:21504\n\t"
      "s_waitcnt lgkmcnt(4)\n\t"
      "v_mfma_f32_32x32x16_bf16 v[176:191], v[28:31], v[220:223], v[176:191]\n\t"
      "ds_read_b128 v[208:211], v244 offset:22528\n\t"
      "s_waitcnt lgkmcnt(4)\n\t"
      "v_mfma_f32_32x32x16_bf16 v[176:191], v[32:35], v[192:195], v[176:191]\n\t"
      "ds_read_b128 v[212:215], v244 offset:23552\n\t"
      "s_waitcnt lgkmcnt(4)\n\t"
      "v_mfma_f32_32x32x16_bf16 v[176:191], v[36:39], v[196:199], v[176:191]\n\t"
      "ds_read2st64_b64 v[216:219], v253 offset0:48 offset1:49\n\t"
      "s_waitcnt lgkmcnt(4)\n\t"
      "v_mfma_f32_32x32x16_bf16 v[160:175], v[48:51], v[200:203], v[160:175]\n\t"
      "ds_read_b128 v[220:223], v246 offset:20480\n\t"
      "s_waitcnt lgkmcnt(4)\n\t"
      "v_mfma_f32_32x32x16_bf16 v[160:175], v[52:55], v[204:207], v[160:175]\n\t"
      "ds_read_b128 v[192:195], v246 offset:21504\n\t"
      "s_waitcnt lgkmcnt(4)\n\t"
      "v_mfma_f32_32x32x16_bf16 v[160:175], v[68:71], v[208:211], v[160:175]\n\t"
      "ds_read_b128 v[196:199], v246 offset:22528\n\t"
      "s_waitcnt lgkmcnt(4)\n\t"
      "v_mfma_f32_32x32x16_bf16 v[160:175], v[72:75], v[212:215], v[160:175]\n\t"
      "ds_read_b128 v[200:203], v246 offset:23552\n\t"
      "s_waitcnt lgkmcnt(4)\n\t"
      "v_mfma_f32_32x32x16_bf16 v[160:175], v[76:79], v[216:219], v[160:175]\n\t"
      "ds_read2st64_b64 v[204:207], v254 offset0:48 offset1:49\n\t"
      "s_waitcnt lgkmcnt(4)\n\t"
      "v_mfma_f32_32x32x16_bf16 v[176:191], v[48:51], v[220:223], v[176:191]\n\t"
      "s_barrier\n\t"
      "v_add_u32 v244, 0x1e000, v244\n\t"
      "v_add_u32 v246, 0x1e000, v246\n\t"
      "v_add_u32 v253, 0x1e000, v253\n\t"
      "v_add_u32 v254, 0x1e000, v254\n\t"
      "ds_read_b128 v[208:211], v246 offset:0\n\t"
      "s_waitcnt lgkmcnt(4)\n\t"
      "v_mfma_f32_32x32x16_bf16 v[176:191], v[52:55], v[192:195], v[176:191]\n\t"
      "ds_read_b128 v[212:215], v246 offset:1024\n\t"
      "s_waitcnt lgkmcnt(4)\n\t"
      "v_mfma_f32_32x32x16_bf16 v[176:191], v[68:71], v[196:199], v[176:191]\n\t"
      "ds_read_b128 v[216:219], v246 offset:2048\n\t"
      "s_waitcnt lgkmcnt(4)\n\t"
      "v_mfma_f32_32x32x16_bf16 v[176:191], v[72:75], v[200:203], v[176:191]\n\t"
      "ds_read_b128 v[220:223], v246 offset:3072\n\t"
      "s_waitcnt lgkmcnt(4)\n\t"
      "v_mfma_f32_32x32x16_bf16 v[176:191], v[76:79], v[204:207], v[176:191]\n\t"
      "ds_read2st64_b64 v[192:195], v254 offset0:8 offset1:9\n\t"
      "s_waitcnt lgkmcnt(4)\n\t"
      "v_mfma_f32_32x32x16_bf16 v[176:191], v[88:91], v[208:211], v[176:191]\n\t"
      "s_barrier\n\t"
      "ds_read_b128 v[196:199], v246 offset:20480\n\t"
      "s_waitcnt lgkmcnt(4)\n\t"
      "v_mfma_f32_32x32x16_bf16 v[176:191], v[92:95], v[212:215], v[176:191]\n\t"
      "ds_read_b128 v[200:203], v246 offset:21504\n\t"
      "s_waitcnt lgkmcnt(4)\n\t"
      "v_mfma_f32_32x32x16_bf16 v[176:191], v[108:111], v[216:219], v[176:191]\n\t"
      "ds_read_b128 v[204:207], v246 offset:22528\n\t"
      "s_waitcnt lgkmcnt(4)\n\t"
      "v_mfma_f32_32x32x16_bf16 v[176:191], v[112:115], v[220:223], v[176:191]\n\t"
      "ds_read_b128 v[208:211], v246 offset:23552\n\t"
      "s_waitcnt lgkmcnt(4)\n\t"
      "v_mfma_f32_32x32x16_bf16 v[176:191], v[116:119], v[192:195], v[176:191]\n\t"
      "ds_read2st64_b64 v[212:215], v254 offset0:48 offset1:49\n\t"
      "s_waitcnt lgkmcnt(4)\n\t"
      "v_mfma_f32_32x32x16_bf16 v[176:191], v[128:131], v[196:199], v[176:191]\n\t"
      "ds_read_b128 v[0:3], v244 offset:0\n\t"
      "s_waitcnt lgkmcnt(4)\n\t"
      "v_mfma_f32_32x32x16_bf16 v[176:191], v[132:135], v[200:203], v[176:191]\n\t"
      "ds_read_b128 v[4:7], v244 offset:1024\n\t"
      "s_waitcnt lgkmcnt(4)\n\t"
      "v_mfma_f32_32x32x16_bf16 v[176:191], v[148:151], v[204:207], v[176:191]\n\t"
      "ds_read_b128 v[8:11], v244 offset:2048\n\t"
      "ds_read_b128 v[12:15], v244 offset:3072\n\t"
      "s_waitcnt lgkmcnt(5)\n\t"
      "v_mfma_f32_32x32x16_bf16 v[176:191], v[152:155], v[208:211], v[176:191]\n\t"
      "ds_read2st64_b64 v[16:19], v253 offset0:8 offset1:9\n\t"
      "ds_read_b128 v[20:23], v244 offset:20480\n\t"
      "s_waitcnt lgkmcnt(6)\n\t"
      "v_mfma_f32_32x32x16_bf16 v[176:191], v[156:159], v[212:215], v[176:191]\n\t"
      "ds_read_b128 v[24:27], v244 offset:21504\n\t"
      "ds_read_b128 v[28:31], v244 offset:22528\n\t"
      "s_waitcnt lgkmcnt(7)\n\t"
      "v_mfma_f32_32x32x16_bf16 v[160:175], v[88:91], v[0:3], v[160:175]\n\t"
      "ds_read_b128 v[32:35], v244 offset:23552\n\t"
      "ds_read2st64_b64 v[36:39], v253 offset0:48 offset1:49\n\t"
      "s_waitcnt lgkmcnt(8)\n\t"
      "v_mfma_f32_32x32x16_bf16 v[160:175], v[92:95], v[4:7], v[160:175]\n\t"
      "s_nop 5\n\t"
      "ds_write_b128 v247, v[176:179] offset:0\n\t"
      "s_waitcnt lgkmcnt(8)\n\t"
      "v_mfma_f32_32x32x16_bf16 v[160:175], v[108:111], v[8:11], v[160:175]\n\t"
      "ds_write_b128 v247, v[180:183] offset:1024\n\t"
      "s_waitcnt lgkmcnt(8)\n\t"
      "v_mfma_f32_32x32x16_bf16 v[160:175], v[112:115], v[12:15], v[160:175]\n\t"
      "ds_write_b128 v247, v[184:187] offset:2048\n\t"
      "s_waitcnt lgkmcnt(8)\n\t"
      "v_mfma_f32_32x32x16_bf16 v[160:175], v[116:119], v[16:19], v[160:175]\n\t"
      "ds_write_b128 v247, v[188:191] offset:3072\n\t"
      "s_waitcnt lgkmcnt(8)\n\t"
      "v_mfma_f32_32x32x16_bf16 v[160:175], v[128:131], v[20:23], v[160:175]\n\t"
      "v_xor_b32 v247, 0x2000, v247\n\t"
      "s_waitcnt lgkmcnt(7)\n\t"
      "v_mfma_f32_32x32x16_bf16 v[160:175], v[132:135], v[24:27], v[160:175]\n\t"
      "s_waitcnt lgkmcnt(6)\n\t"
      "v_mfma_f32_32x32x16_bf16 v[160:175], v[148:151], v[28:31], v[160:175]\n\t"
      "s_waitcnt lgkmcnt(5)\n\t"
      "v_mfma_f32_32x32x16_bf16 v[160:175], v[152:155], v[32:35], v[160:175]\n\t"
      "s_waitcnt lgkmcnt(0)\n\t"
      "s_barrier\n\t"
      "ds_read_b128 v[176:179], v247 offset:0\n\t"
      "ds_read_b128 v[180:183], v247 offset:1024\n\t"
      "ds_read_b128 v[184:187], v247 offset:2048\n\t"
      "ds_read_b128 v[188:191], v247 offset:3072\n\t"
      "v_mfma_f32_32x32x16_bf16 v[160:175], v[156:159], v[36:39], v[160:175]\n\t"
      "s_nop 10\n\t"
      "s_waitcnt lgkmcnt(0)\n\t"
      "v_pk_add_f32 v[160:161], v[160:161], v[176:177]\n\t"
      "v_pk_add_f32 v[162:163], v[162:163], v[178:179]\n\t"
      "v_cvt_pk_bf16_f32 v192, v160, v161\n\t"
      "v_cvt_pk_bf16_f32 v193, v162, v163\n\t"
      "v_pk_add_f32 v[164:165], v[164:165], v[180:181]\n\t"
      "v_pk_add_f32 v[166:167], v[166:167], v[182:183]\n\t"
      "v_cvt_pk_bf16_f32 v194, v164, v165\n\t"
      "v_cvt_pk_bf16_f32 v195, v166, v167\n\t"
      "ds_write_b128 v250, v[192:195]\n\t"
      "v_pk_add_f32 v[168:169], v[168:169], v[184:185]\n\t"
      "v_pk_add_f32 v[170:171], v[170:171], v[186:187]\n\t"
      "v_cvt_pk_bf16_f32 v196, v168, v169\n\t"
      "v_cvt_pk_bf16_f32 v197, v170, v171\n\t"
      "v_pk_add_f32 v[172:173], v[172:173], v[188:189]\n\t"
      "v_pk_add_f32 v[174:175], v[174:175], v[190:191]\n\t"
      "v_cvt_pk_bf16_f32 v198, v172, v173\n\t"
      "v_cvt_pk_bf16_f32 v199, v174, v175\n\t"
      "ds_write_b128 v251, v[196:199]\n\t"
      "ds_read_b128 v[200:203], v247 offset:16384\n\t"
      "ds_read_b128 v[204:207], v247 offset:17408\n\t"
      "s_movk_i32 %[t0], 0x5000\n\t"
      "s_waitcnt lgkmcnt(1)\n\t"
      "buffer_store_dwordx4 v[200:203], v248, %[srdout], 0 offen nt\n\t"
      "s_waitcnt lgkmcnt(0)\n\t"
      "buffer_store_dwordx4 v[204:207], v248, %[srdout], %[t0] offen nt\n\t"
      : "+{v[230:231]}"(in0), "+{v[232:239]}"(in), "+{v[240:247]}"(in2), "+{v[248:255]}"(in3), [c] "=&s"(c), [it] "=&s"(it), [t0] "=&s"(t0), [t1] "=&s"(t1),
        [p0] "=&s"(p0)
      : "{s[68:69]}"(tag), [srdo] "s"(srdo), [srdws] "s"(srdws), [hw1k] "s"(hw1k), [skv] "s"(skv), [first] "s"(first),
        [mwt] "s"(mwt), [job] "s"(job), [ostep] "s"(ostep), [dtr] "s"(dtr), [dts] "s"(dts), [sdst] "s"(sdst), [sfw] "s"(sfw),
        [st4] "s"(st4), [sw2] "s"(sw2), [mox] "s"(mox), [xrot] "s"(xrot), [so] "s"(so), [srdout] "s"(srdout), [srdc] "s"(srdc)
      : "memory", "scc", "vcc", "v0","v1","v2","v3","v4","v5","v6","v7","v8","v9","v10","v11","v12","v13","v14","v15","v16","v17","v18","v19","v20","v21","v22","v23",
        "v24","v25","v26","v27","v28","v29","v30","v31","v32","v33","v34","v35","v36","v37","v38","v39","v40","v41","v42","v43","v44","v45","v46","v47",
        "v48","v49","v50","v51","v52","v53","v54","v55","v56","v57","v58","v59","v60","v61","v62","v63","v64","v65","v66","v67","v68","v69","v70","v71",
        "v72","v73","v74","v75","v76","v77","v78","v79","v80","v81","v82","v83","v84","v85","v86","v87","v88","v89","v90","v91","v92","v93","v94","v95",
        "v96","v97","v98","v99","v100","v101","v102","v103","v104","v105","v106","v107","v108","v109","v110","v111","v112","v113","v114","v115","v116","v117","v118","v119",
        "v120","v121","v122","v123","v124","v125","v126","v127","v128","v129","v130","v131","v132","v133","v134","v135","v136","v137","v138","v139","v140","v141","v142","v143",
        "v144","v145","v146","v147","v148","v149","v150","v151","v152","v153","v154","v155","v156","v157","v158","v159","v160","v161","v162","v163","v164","v165","v166","v167",
        "v168","v169","v170","v171","v172","v173","v174","v175","v176","v177","v178","v179","v180","v181","v182","v183","v184","v185","v186","v187","v188","v189","v190","v191",
        "v192","v193","v194","v195","v196","v197","v198","v199","v200","v201","v202","v203","v204","v205","v206","v207","v208","v209","v210","v211","v212","v213","v214","v215",
        "v216","v217","v218","v219","v220","v221","v222","v223","v224","v225","v226","v227","v228","v229");

  } else {
    u64 tag;
    const unsigned t = lane & 31, kvb = WS_KV + b * KV_BR;
    const unsigned fo = WS_FO + (wid * 4 + w) * 256;
    {
      const unsigned P = w & 1, Kh = w >> 1, job = w != 0, jj = wid & 7, ju = wid >> 3, mwt = L_WT + w * 5120;
      unsigned hv[20];
      const unsigned k1 = g4 >> 1, k0 = g4 & 1, cq = 8 * (4 * (p & 1) + 2 * k0 + (p >> 1));
      hv[8] = sel(job, (160 * (ju & 3) + (lane >> 1)) * 1280 + 1024 + 32 * jj + (lane & 1) * 16, (80 * ((ju >> 1) & 7) + (lane >> 2)) * 1280 + 128 * jj + 64 * (ju & 1) + p * 16);
      hv[9] = sel(lane < 8, WS_FO + ((tb * 16 + 2 * lane + Kh) * 4 + 2 * b + P) * 256, OOB);
      hv[10] = mwt + sel(job, (20 * k1 + 4 * k0 + q) * 32 + 8 * p, (4 * h + q) * 64 + cq);
      hv[11] = mwt + sel(job, (80 * k1 + 16 + 2 * k0 + 20 * (q >> 1) + (q & 1)) * 32 + 8 * p, (16 + 2 * h + 20 * (q >> 1) + (q & 1)) * 64 + cq);
      hv[12] = L_OX + Kh * 15360 + vlin;
      hv[14] = L_OX + (Kh ^ 1) * 10240 + Kh * 5120 + vlin;
      hv[15] = L_RED + (w ^ 2) * 4096 + vlin;
      hv[13] = sel(lane < 16, WS_KVF + (b * 128 + lane) * 64, OOB);
      hv[16] = (tb * 128 + 64 * b + 32 * Kh + (lane >> 2)) * 1280 + 128 * hg + 64 * P + 16 * (p ^ ((lane >> 3) & 3));
      hv[18] = L_RED + 16384 + w * 4096 + t * 64 + ((2 * h) ^ ((t >> 1) & 3)) * 16;
      hv[19] = hv[18] ^ 16;
      hv[17] = WS_W2F + hg * 4096 + lane * 64;
      u32x4* d = (u32x4*)(lds + L_QS + 2 * QSLOT + 12288 + w * 3072) + lane;
      d[0] = u32x4{hv[8], hv[9], hv[10], hv[11]}; d[64] = u32x4{hv[12], hv[13], hv[14], hv[15]}; d[128] = u32x4{hv[16], hv[17], hv[18], hv[19]};
    }
    u32x2 cc;
    cc[0] = vlin; cc[1] = WS_OF + (tb * 4 + w) * 81920 + col * 5120 + 2048 + lane * 8;
    asm volatile("" ::: "memory");
    KARG_WAIT();
    char* const ws = KARG(ah[6], ah[7]);
    asm volatile("s_load_dwordx2 s[68:69], %1, 0x0" : "=&{s[68:69]}"(tag) : "s"(ws));
    const rsrc_t srdws = mk(ws, WS_SIZE), srdout = mk(KARG(ah[4], ah[5]), 2048 * 1280);
    unsigned fx;
    u32x4 q0, q1, q2, q3; u32x2 r0, r1;
    {
      u32x16 in;
      const unsigned tp = sel(isv, (t & 19) | ((t & 4) << 1) | ((t & 8) >> 1), t), mqd = 4 * (g4 & 1) + p;
      in[0] = L_XR + tp * 128 + (((2 * w + h) ^ ((tp >> 1) & 7)) * 16);
      in[1] = L_W + (16 * w + 8 * h + q) * 40 + 8 * (mqd < 4 ? mqd : 4);
      in[2] = L_G + 32 * w + 16 * h;
      in[3] = L_STG + w * 1024 + vlin;
      const unsigned kt = tb * 4 + wkv, pb = kvb + (kt >> 1) * PB, par = kt & 1, r = lane >> 1;
      const unsigned cs = sel(isv & (r >= 16), 16 + ((0x75326410u >> (4 * (r & 7))) & 7), r);
      const bool kx = (isv == 0) & (w >= 2);
      in[4] = L_STG + sel(kx, 2 * 4224 + vlin, (sel(isv, w & 2, 0) + (lane & 1)) * 4224 + (32 * (w & 1) + cs) * 16);
      const unsigned offK = sel((w < 2) | ((w == 2) & (h == 0)), pb + par * 1024 + sel(w < 2, 2048 + w * 512, 0) + lane * 8, OOB);
      const unsigned offV = sel(r < 24, pb + sel(par, 4096 + (w >> 1) * 768, 256 + (w >> 1) * 1024) + (w & 1) * 384 + lane * 8, OOB);
      in[5] = isv ? offV : offK;
      in[6] = sel((isv == 0) & (w == 2) & (h == 1), WS_GF + (b * 2048 + kt * 32 + t) * 4, OOB);
      in[7] = (isv & (cs >= 20)) ? 0u : ~0u;
      in[8] = (isv & ((cs == 20) | (cs == 21))) ? ~0u : 0u;
      const bool own = w == wkv;
      const unsigned xb = sel(own, L_XR + c0 * 4096, L_QS + (w - (w > wkv)) * 4096);
      const unsigned base = L_QS + 12288 + (8 * g4 + q) * 192 + (g4 & 1) * 32, pq = ((p & 1) << 1) | (p >> 1);
      in[10] = base + 8 * pq;
      in[11] = base + 32 + 40 * pq;
      in[13] = sel((w == 0) & (lane < 8), WS_KVF + (b * 128 + tb * 8 + hg) * 64 + lane * 8, OOB);
      in[14] = xb + (lane & 15) * 128 + ((g4 ^ ((lane >> 1) & 7)) * 16);
      unsigned xp = sel(own, c0 * 4096, 0), it;
      const unsigned xstep = own ? 4096 : QSLOT, xend = own ? 40960 : 4 * QSLOT, qslot = QSLOT;
      const unsigned gsc = b ? 0x3fb8aa3bu : 0xbfb8aa3bu;
      const unsigned ksc = isv ? 0x3f800000u : 0x3ea52b5cu;
      const unsigned done = 163344 + 4 * w, chsz = 2 * KVCH, lo = 0x33800000u, span = 0x3e000000u;
      u32x16 ab;
      ab[1] = L_KV0 + vlin; ab[2] = L_KV0 + t * 8; ab[3] = L_KV0 + vrow(lane); ab[4] = WS_GF + (b * 2048 + tb * 128 + w * 32 + t) * 4;
      ab[5] = WS_OF + (tb * 4 + w) * 81920 + col * 5120 + vlin;
      ab[12] = sel(lane == 0, fo, OOB);
      ab[13] = sel(lane < 32, fo + lane * 8, OOB);
      ab[15] = 163328 + 4 * p;
      *(unsigned*)(lds + 163328 + 4 * (lane & 7)) = 0;
      u64 badx, bady;
      asm volatile(
        "v_xor_b32 v215, 64, v214\n\t"
        "v_mov_b32 v222, 0\n\t"
        "v_mov_b32 v223, 0\n\t"
        "v_mov_b32 v216, 0xdddddddd\n\t"
        "s_nop 0\n\t"
        "v_mfma_f32_32x32x4_2b_bf16 v[96:127], v[222:223], v[222:223], 0\n\t"
        "v_mfma_f32_32x32x4_2b_bf16 v[128:159], v[222:223], v[222:223], 0\n\t"
        "s_waitcnt lgkmcnt(0)\n\t"
        "v_mov_b32 v198, s68\n\t"
        "v_mov_b32 v199, s69\n\t"
        "s_add_u32 s68, s68, 1\n\t"
        "s_addc_u32 s69, s69, 0\n\t"
        "v_mov_b32 v196, s68\n\t"
        "v_mov_b32 v197, s69\n\t"
        "s_mov_b32 s70, 0xfff00000\n\t"
        "s_mov_b32 s71, 0xfff00000\n\t"
        "s_cmp_lg_u32 %[isv], 0\n\t"
        "s_cbranch_scc1 11f\n\t"
        "s_barrier\n\t"
        "ds_read_b128 v[0:3], v200 offset:0\n\t"
        "ds_read_b64_tr_b16 v[40:41], v201 offset:0\n\t"
        "ds_read_b64_tr_b16 v[42:43], v201 offset:160\n\t"
        "s_mov_b64 exec, s[70:71]\n\t"
        "ds_read_b128 v[40:43], v202 offset:0\n\t"
        "s_mov_b64 exec, -1\n\t"
        "ds_read_b128 v[4:7], v200 offset:4096\n\t"
        "ds_read_b64_tr_b16 v[44:45], v201 offset:2560\n\t"
        "ds_read_b64_tr_b16 v[46:47], v201 offset:2720\n\t"
        "s_mov_b64 exec, s[70:71]\n\t"
        "ds_read_b128 v[44:47], v202 offset:128\n\t"
        "s_mov_b64 exec, -1\n\t"
        "ds_read_b128 v[8:11], v200 offset:8192\n\t"
        "ds_read_b64_tr_b16 v[48:49], v201 offset:5120\n\t"
        "ds_read_b64_tr_b16 v[50:51], v201 offset:5280\n\t"
        "s_mov_b64 exec, s[70:71]\n\t"
        "ds_read_b128 v[48:51], v202 offset:256\n\t"
        "s_mov_b64 exec, -1\n\t"
        "ds_read_b128 v[12:15], v200 offset:12288\n\t"
        "ds_read_b64_tr_b16 v[52:53], v201 offset:7680\n\t"
        "ds_read_b64_tr_b16 v[54:55], v201 offset:7840\n\t"
        "s_mov_b64 exec, s[70:71]\n\t"
        "ds_read_b128 v[52:55], v202 offset:384\n\t"
        "s_mov_b64 exec, -1\n\t"
        "s_waitcnt lgkmcnt(12)\n\t"
        "v_mfma_f32_32x32x16_bf16 v[80:95], v[40:43], v[0:3], 0\n\t"
        "ds_read_b128 v[16:19], v200 offset:16384\n\t"
        "ds_read_b64_tr_b16 v[56:57], v201 offset:10240\n\t"
        "ds_read_b64_tr_b16 v[58:59], v201 offset:10400\n\t"
        "s_mov_b64 exec, s[70:71]\n\t"
        "ds_read_b128 v[56:59], v202 offset:512\n\t"
        "s_mov_b64 exec, -1\n\t"
        "s_waitcnt lgkmcnt(12)\n\t"
        "v_mfma_f32_32x32x16_bf16 v[80:95], v[44:47], v[4:7], v[80:95]\n\t"
        "ds_read_b128 v[20:23], v200 offset:20480\n\t"
        "ds_read_b64_tr_b16 v[60:61], v201 offset:12800\n\t"
        "ds_read_b64_tr_b16 v[62:63], v201 offset:12960\n\t"
        "s_mov_b64 exec, s[70:71]\n\t"
        "ds_read_b128 v[60:63], v202 offset:640\n\t"
        "s_mov_b64 exec, -1\n\t"
        "s_waitcnt lgkmcnt(12)\n\t"
        "v_mfma_f32_32x32x16_bf16 v[80:95], v[48:51], v[8:11], v[80:95]\n\t"
        "ds_read_b128 v[24:27], v200 offset:24576\n\t"
        "ds_read_b64_tr_b16 v[64:65], v201 offset:15360\n\t"
        "ds_read_b64_tr_b16 v[66:67], v201 offset:15520\n\t"
        "s_mov_b64 exec, s[70:71]\n\t"
        "ds_read_b128 v[64:67], v202 offset:768\n\t"
        "s_mov_b64 exec, -1\n\t"
        "s_waitcnt lgkmcnt(12)\n\t"
        "v_mfma_f32_32x32x16_bf16 v[80:95], v[52:55], v[12:15], v[80:95]\n\t"
        "ds_read_b128 v[28:31], v200 offset:28672\n\t"
        "ds_read_b64_tr_b16 v[68:69], v201 offset:17920\n\t"
        "ds_read_b64_tr_b16 v[70:71], v201 offset:18080\n\t"
        "s_mov_b64 exec, s[70:71]\n\t"
        "ds_read_b128 v[68:71], v202 offset:896\n\t"
        "s_mov_b64 exec, -1\n\t"
        "s_waitcnt lgkmcnt(12)\n\t"
        "v_mfma_f32_32x32x16_bf16 v[80:95], v[56:59], v[16:19], v[80:95]\n\t"
        "ds_read_b128 v[32:35], v200 offset:32768\n\t"
        "ds_read_b64_tr_b16 v[72:73], v201 offset:20480\n\t"
        "ds_read_b64_tr_b16 v[74:75], v201 offset:20640\n\t"
        "s_mov_b64 exec, s[70:71]\n\t"
        "ds_read_b128 v[72:75], v202 offset:1024\n\t"
        "s_mov_b64 exec, -1\n\t"
        "s_waitcnt lgkmcnt(12)\n\t"
        "v_mfma_f32_32x32x16_bf16 v[80:95], v[60:63], v[20:23], v[80:95]\n\t"
        "ds_read_b128 v[36:39], v200 offset:36864\n\t"
        "ds_read_b64_tr_b16 v[76:77], v201 offset:23040\n\t"
        "ds_read_b64_tr_b16 v[78:79], v201 offset:23200\n\t"
        "s_mov_b64 exec, s[70:71]\n\t"
        "ds_read_b128 v[76:79], v202 offset:1152\n\t"
        "s_mov_b64 exec, -1\n\t"
        "s_waitcnt lgkmcnt(12)\n\t"
        "v_mfma_f32_32x32x16_bf16 v[80:95], v[64:67], v[24:27], v[80:95]\n\t"
        "s_waitcnt lgkmcnt(8)\n\t"
        "v_mfma_f32_32x32x16_bf16 v[80:95], v[68:71], v[28:31], v[80:95]\n\t"
        "s_waitcnt lgkmcnt(4)\n\t"
        "v_mfma_f32_32x32x16_bf16 v[80:95], v[72:75], v[32:35], v[80:95]\n\t"
        "s_waitcnt lgkmcnt(0)\n\t"
        "v_mfma_f32_32x32x16_bf16 v[80:95], v[76:79], v[36:39], v[80:95]\n\t"
        "s_nop 11\n\t"
        "ds_write_b128 v203, v[80:83] offset:0\n\t"
        "ds_write_b128 v203, v[84:87] offset:4224\n\t"
        "ds_write_b128 v203, v[88:91] offset:8448\n\t"
        "s_branch 12f\n\t"
        "11:\n\t"
        "s_barrier\n\t"
        "ds_read_b128 v[0:3], v200 offset:0\n\t"
        "ds_read_b64_tr_b16 v[40:41], v201 offset:0\n\t"
        "ds_read_b64_tr_b16 v[42:43], v201 offset:160\n\t"
        "ds_read_b128 v[4:7], v200 offset:4096\n\t"
        "ds_read_b64_tr_b16 v[44:45], v201 offset:2560\n\t"
        "ds_read_b64_tr_b16 v[46:47], v201 offset:2720\n\t"
        "ds_read_b128 v[8:11], v200 offset:8192\n\t"
        "ds_read_b64_tr_b16 v[48:49], v201 offset:5120\n\t"
        "ds_read_b64_tr_b16 v[50:51], v201 offset:5280\n\t"
        "ds_read_b128 v[12:15], v200 offset:12288\n\t"
        "ds_read_b64_tr_b16 v[52:53], v201 offset:7680\n\t"
        "ds_read_b64_tr_b16 v[54:55], v201 offset:7840\n\t"
        "s_waitcnt lgkmcnt(9)\n\t"
        "v_mfma_f32_32x32x16_bf16 v[80:95], v[0:3], v[40:43], 0\n\t"
        "ds_read_b128 v[16:19], v200 offset:16384\n\t"
        "ds_read_b64_tr_b16 v[56:57], v201 offset:10240\n\t"
        "ds_read_b64_tr_b16 v[58:59], v201 offset:10400\n\t"
        "s_waitcnt lgkmcnt(9)\n\t"
        "v_mfma_f32_32x32x16_bf16 v[80:95], v[4:7], v[44:47], v[80:95]\n\t"
        "ds_read_b128 v[20:23], v200 offset:20480\n\t"
        "ds_read_b64_tr_b16 v[60:61], v201 offset:12800\n\t"
        "ds_read_b64_tr_b16 v[62:63], v201 offset:12960\n\t"
        "s_waitcnt lgkmcnt(9)\n\t"
        "v_mfma_f32_32x32x16_bf16 v[80:95], v[8:11], v[48:51], v[80:95]\n\t"
        "ds_read_b128 v[24:27], v200 offset:24576\n\t"
        "ds_read_b64_tr_b16 v[64:65], v201 offset:15360\n\t"
        "ds_read_b64_tr_b16 v[66:67], v201 offset:15520\n\t"
        "s_waitcnt lgkmcnt(9)\n\t"
        "v_mfma_f32_32x32x16_bf16 v[80:95], v[12:15], v[52:55], v[80:95]\n\t"
        "ds_read_b128 v[28:31], v200 offset:28672\n\t"
        "ds_read_b64_tr_b16 v[68:69], v201 offset:17920\n\t"
        "ds_read_b64_tr_b16 v[70:71], v201 offset:18080\n\t"
        "s_waitcnt lgkmcnt(9)\n\t"
        "v_mfma_f32_32x32x16_bf16 v[80:95], v[16:19], v[56:59], v[80:95]\n\t"
        "ds_read_b128 v[32:35], v200 offset:32768\n\t"
        "ds_read_b64_tr_b16 v[72:73], v201 offset:20480\n\t"
        "ds_read_b64_tr_b16 v[74:75], v201 offset:20640\n\t"
        "s_waitcnt lgkmcnt(9)\n\t"
        "v_mfma_f32_32x32x16_bf16 v[80:95], v[20:23], v[60:63], v[80:95]\n\t"
        "ds_read_b128 v[36:39], v200 offset:36864\n\t"
        "ds_read_b64_tr_b16 v[76:77], v201 offset:23040\n\t"
        "ds_read_b64_tr_b16 v[78:79], v201 offset:23200\n\t"
        "s_waitcnt lgkmcnt(9)\n\t"
        "v_mfma_f32_32x32x16_bf16 v[80:95], v[24:27], v[64:67], v[80:95]\n\t"
        "s_waitcnt lgkmcnt(6)\n\t"
        "v_mfma_f32_32x32x16_bf16 v[80:95], v[28:31], v[68:71], v[80:95]\n\t"
        "s_waitcnt lgkmcnt(3)\n\t"
        "v_mfma_f32_32x32x16_bf16 v[80:95], v[32:35], v[72:75], v[80:95]\n\t"
        "s_waitcnt lgkmcnt(0)\n\t"
        "v_mfma_f32_32x32x16_bf16 v[80:95], v[36:39], v[76:79], v[80:95]\n\t"
        "s_nop 11\n\t"
        "ds_write_b128 v203, v[80:83] offset:0\n\t"
        "ds_write_b128 v203, v[84:87] offset:4224\n\t"
        "ds_write_b128 v203, v[88:91] offset:8448\n\t"
        "ds_write_b128 v203, v[92:95] offset:12672\n\t"
        "12:\n\t"
        "s_mov_b32 %[it], 0\n\t"
        "s_waitcnt lgkmcnt(0)\n\t"
        "s_barrier\n\t"
        "ds_read_b128 v[72:75], v204 offset:0\n\t"
        "ds_read_b128 v[76:79], v204 offset:1024\n\t"
        "ds_read_b128 v[80:83], v204 offset:2048\n\t"
        "ds_read_b128 v[84:87], v204 offset:3072\n\t"
        "ds_read_b128 v[0:3], v214 offset:0\n\t"
        "ds_read_b128 v[4:7], v214 offset:2048\n\t"
        "ds_read_b64_tr_b16 v[8:9], v210 offset:0\n\t"
        "ds_read_b64_tr_b16 v[10:11], v210 offset:768\n\t"
        "ds_read_b64_tr_b16 v[12:13], v210 offset:40\n\t"
        "ds_read_b64_tr_b16 v[14:15], v210 offset:808\n\t"
        "ds_read_b64_tr_b16 v[16:17], v210 offset:80\n\t"
        "ds_read_b64_tr_b16 v[18:19], v210 offset:848\n\t"
        "ds_read_b64_tr_b16 v[20:21], v210 offset:120\n\t"
        "ds_read_b64_tr_b16 v[22:23], v210 offset:888\n\t"
        "s_waitcnt lgkmcnt(10)\n\t"
        "v_pk_add_f32 v[72:73], v[72:73], v[76:77]\n\t"
        "v_pk_add_f32 v[80:81], v[80:81], v[84:85]\n\t"
        "v_pk_add_f32 v[74:75], v[74:75], v[78:79]\n\t"
        "v_pk_add_f32 v[82:83], v[82:83], v[86:87]\n\t"
        "v_pk_add_f32 v[72:73], v[72:73], v[80:81]\n\t"
        "v_pk_add_f32 v[74:75], v[74:75], v[82:83]\n\t"
        "v_mul_f32 v76, %[gsc], v72\n\t"
        "v_exp_f32 v76, v76\n\t"
        "v_cvt_pk_bf16_f32 v78, v72, v73\n\t"
        "v_cvt_pk_bf16_f32 v79, v74, v75\n\t"
        "v_add_f32 v76, 1.0, v76\n\t"
        "v_rcp_f32 v76, v76\n\t"
        "v_and_b32 v78, v78, v207\n\t"
        "v_and_b32 v79, v79, v207\n\t"
        "v_lshlrev_b32 v72, 16, v78\n\t"
        "v_and_b32 v73, 0xffff0000, v78\n\t"
        "v_lshlrev_b32 v74, 16, v79\n\t"
        "v_and_b32 v75, 0xffff0000, v79\n\t"
        "v_and_b32 v80, 0x7fff7fff, v78\n\t"
        "v_and_b32 v81, 0x7fff7fff, v79\n\t"
        "v_pk_max_u16 v80, v80, v81\n\t"
        "s_movk_i32 %[t0], 0x4b80\n\t"
        "v_pk_max_u16 v80, v80, v80 op_sel:[0,1] op_sel_hi:[1,0]\n\t"
        "v_mul_f32 v72, %[ksc], v72\n\t"
        "v_mul_f32 v73, %[ksc], v73\n\t"
        "v_mul_f32 v74, %[ksc], v74\n\t"
        "v_mul_f32 v75, %[ksc], v75\n\t"
        "v_cmp_le_u16 vcc, %[t0], v80\n\t"
        "v_cvt_pk_bf16_f32 v78, v72, v73\n\t"
        "v_cvt_pk_bf16_f32 v79, v74, v75\n\t"
        "s_cmp_lg_u64 vcc, 0\n\t"
        "s_cselect_b32 %[t0], -1, 0x3f803f80\n\t"
        "v_and_or_b32 v78, v208, %[t0], v78\n\t"
        "v_and_or_b32 v79, v208, %[t0], v79\n\t"
        "buffer_store_dwordx2 v[78:79], v205, %[srdws], 0 offen sc1\n\t"
        "buffer_store_dword v76, v206, %[srdws], 0 offen sc1\n\t"
        "buffer_store_dwordx2 v[198:199], v229, %[srdws], 0 offen sc1\n\t"
        "ds_read_b64_tr_b16 v[24:25], v211 offset:0\n\t"
        "ds_read_b64_tr_b16 v[26:27], v211 offset:768\n\t"
        "s_waitcnt lgkmcnt(8)\n\t"
        "20:\n\t"
        "v_mfma_f32_16x16x32_bf16 v[96:99], v[8:11], v[0:3], v[96:99]\n\t"
        "s_add_u32 %[it], %[it], 1\n\t"
        "ds_read_b128 v[28:31], v215 offset:0\n\t"
        "ds_read_b128 v[32:35], v215 offset:2048\n\t"
        "v_mfma_f32_16x16x32_bf16 v[100:103], v[8:11], v[4:7], v[100:103]\n\t"
        "s_waitcnt lgkmcnt(8)\n\t"
        "ds_read_b64_tr_b16 v[36:37], v210 offset:6144\n\t"
        "ds_read_b64_tr_b16 v[38:39], v210 offset:6912\n\t"
        "v_mfma_f32_16x16x32_bf16 v[104:107], v[12:15], v[0:3], v[104:107]\n\t"
        "s_and_b32 %[t0], %[it], 3\n\t"
        "ds_read_b64_tr_b16 v[40:41], v210 offset:6184\n\t"
        "ds_read_b64_tr_b16 v[42:43], v210 offset:6952\n\t"
        "v_mfma_f32_16x16x32_bf16 v[108:111], v[12:15], v[4:7], v[108:111]\n\t"
        "s_waitcnt lgkmcnt(10)\n\t"
        "ds_read_b64_tr_b16 v[44:45], v210 offset:6224\n\t"
        "ds_read_b64_tr_b16 v[46:47], v210 offset:6992\n\t"
        "v_mfma_f32_16x16x32_bf16 v[112:115], v[16:19], v[0:3], v[112:115]\n\t"
        "s_cselect_b32 %[t0], %[qslot], 0xfffee000\n\t"
        "ds_read_b64_tr_b16 v[48:49], v210 offset:6264\n\t"
        "ds_read_b64_tr_b16 v[50:51], v210 offset:7032\n\t"
        "v_mfma_f32_16x16x32_bf16 v[116:119], v[16:19], v[4:7], v[116:119]\n\t"
        "s_waitcnt lgkmcnt(10)\n\t"
        "ds_read_b64_tr_b16 v[52:53], v211 offset:6144\n\t"
        "ds_read_b64_tr_b16 v[54:55], v211 offset:6912\n\t"
        "v_mfma_f32_16x16x32_bf16 v[120:123], v[20:23], v[0:3], v[120:123]\n\t"
        "s_add_u32 %[xp], %[xp], %[xstep]\n\t"
        "s_cmp_eq_u32 %[xp], %[xend]\n\t"
        "s_cselect_b32 %[xp], 0, %[xp]\n\t"
        "v_mfma_f32_16x16x32_bf16 v[124:127], v[20:23], v[4:7], v[124:127]\n\t"
        "s_cselect_b32 %[t1], %[xend], 0\n\t"
        "v_add_u32 v210, %[t0], v210\n\t"
        "v_add_u32 v211, %[t0], v211\n\t"
        "v_mfma_f32_16x16x32_bf16 v[128:131], v[24:27], v[0:3], v[128:131]\n\t"
        "s_sub_u32 %[t1], %[xstep], %[t1]\n\t"
        "v_add_u32 v214, %[t1], v214\n\t"
        "v_add_u32 v215, %[t1], v215\n\t"
        "v_mfma_f32_16x16x32_bf16 v[132:135], v[24:27], v[4:7], v[132:135]\n\t"
        "s_cmp_lg_u32 %[it], 3\n\t"
        "s_waitcnt lgkmcnt(8)\n\t"
        "v_mfma_f32_16x16x32_bf16 v[96:99], v[36:39], v[28:31], v[96:99]\n\t"
        "s_cbranch_scc0 22f\n\t"
        "s_cmp_eq_u32 %[it], 10\n\t"
        "s_waitcnt lgkmcnt(0)\n\t"
        "v_mfma_f32_16x16x32_bf16 v[100:103], v[36:39], v[32:35], v[100:103]\n\t"
        "s_barrier\n\t"
        "s_cbranch_scc1 24f\n\t"
        "23:\n\t"
        "v_mfma_f32_16x16x32_bf16 v[104:107], v[40:43], v[28:31], v[104:107]\n\t"
        "ds_read_b128 v[0:3], v214 offset:0\n\t"
        "ds_read_b128 v[4:7], v214 offset:2048\n\t"
        "v_mfma_f32_16x16x32_bf16 v[108:111], v[40:43], v[32:35], v[108:111]\n\t"
        "ds_read_b64_tr_b16 v[8:9], v210 offset:0\n\t"
        "ds_read_b64_tr_b16 v[10:11], v210 offset:768\n\t"
        "v_mfma_f32_16x16x32_bf16 v[112:115], v[44:47], v[28:31], v[112:115]\n\t"
        "ds_read_b64_tr_b16 v[12:13], v210 offset:40\n\t"
        "ds_read_b64_tr_b16 v[14:15], v210 offset:808\n\t"
        "v_mfma_f32_16x16x32_bf16 v[116:119], v[44:47], v[32:35], v[116:119]\n\t"
        "ds_read_b64_tr_b16 v[16:17], v210 offset:80\n\t"
        "ds_read_b64_tr_b16 v[18:19], v210 offset:848\n\t"
        "v_mfma_f32_16x16x32_bf16 v[120:123], v[48:51], v[28:31], v[120:123]\n\t"
        "ds_read_b64_tr_b16 v[20:21], v210 offset:120\n\t"
        "ds_read_b64_tr_b16 v[22:23], v210 offset:888\n\t"
        "v_mfma_f32_16x16x32_bf16 v[124:127], v[48:51], v[32:35], v[124:127]\n\t"
        "ds_read_b64_tr_b16 v[24:25], v211 offset:0\n\t"
        "ds_read_b64_tr_b16 v[26:27], v211 offset:768\n\t"
        "v_mfma_f32_16x16x32_bf16 v[128:131], v[52:55], v[28:31], v[128:131]\n\t"
        "s_waitcnt lgkmcnt(8)\n\t"
        "v_mfma_f32_16x16x32_bf16 v[132:135], v[52:55], v[32:35], v[132:135]\n\t"
        "s_branch 20b\n\t"
        "22:\n\t"
        "v_mfma_f32_16x16x32_bf16 v[100:103], v[36:39], v[32:35], v[100:103]\n\t"
        "s_waitcnt vmcnt(0) lgkmcnt(0)\n\t"
        "s_barrier\n\t"
        "buffer_store_dwordx2 v[196:197], v213, %[srdws], 0 offen sc1\n\t"
        "s_branch 23b\n\t"
        "30:\n\t"
        "s_mov_b32 %[t1], 1048576\n\t"
        "36:\n\t"
        "ds_read_b32 v229, v231 offset:0\n\t"
        "s_waitcnt lgkmcnt(0)\n\t"
        "v_cmp_gt_u32 vcc, 1, v229\n\t"
        "s_cmp_eq_u64 vcc, 0\n\t"
        "s_cbranch_scc1 37f\n\t"
        "s_sleep 1\n\t"
        "s_sub_u32 %[t1], %[t1], 1\n\t"
        "s_cmp_lg_u32 %[t1], 0\n\t"
        "s_cbranch_scc1 36b\n\t"
        "37:\n\t"
        "s_branch 31f\n\t"
        "32:\n\t"
        "s_mov_b32 %[t1], 1048576\n\t"
        "38:\n\t"
        "ds_read_b32 v229, v231 offset:0\n\t"
        "s_waitcnt lgkmcnt(0)\n\t"
        "v_cmp_gt_u32 vcc, %[xp], v229\n\t"
        "s_cmp_eq_u64 vcc, 0\n\t"
        "s_cbranch_scc1 39f\n\t"
        "s_sleep 1\n\t"
        "s_sub_u32 %[t1], %[t1], 1\n\t"
        "s_cmp_lg_u32 %[t1], 0\n\t"
        "s_cbranch_scc1 38b\n\t"
        "39:\n\t"
        "s_branch 1f\n\t"
        "24:\n\t"
        "v_mfma_f32_16x16x32_bf16 v[128:131], v[52:55], v[28:31], v[128:131]\n\t"
        "ds_read2st64_b64 v[188:191], v218 offset0:0 offset1:2\n\t"
        "ds_read_b128 v[180:183], v217 offset:2048\n\t"
        "v_mfma_f32_16x16x32_bf16 v[132:135], v[52:55], v[32:35], v[132:135]\n\t"
        "ds_read_b128 v[184:187], v217 offset:3072\n\t"
        "s_nop 0\n\t"
        "v_mfma_f32_16x16x32_bf16 v[104:107], v[40:43], v[28:31], v[104:107]\n\t"
        "s_nop 0\n\t"
        "v_mfma_f32_16x16x32_bf16 v[108:111], v[40:43], v[32:35], v[108:111]\n\t"
        "v_cvt_pk_bf16_f32 v160, v96, v97\n\t"
        "v_cvt_pk_bf16_f32 v161, v98, v99\n\t"
        "v_mfma_f32_16x16x32_bf16 v[112:115], v[44:47], v[28:31], v[112:115]\n\t"
        "v_cvt_pk_bf16_f32 v162, v100, v101\n\t"
        "v_cvt_pk_bf16_f32 v163, v102, v103\n\t"
        "v_mfma_f32_16x16x32_bf16 v[116:119], v[44:47], v[32:35], v[116:119]\n\t"
        "v_cvt_pk_bf16_f32 v176, v128, v129\n\t"
        "v_cvt_pk_bf16_f32 v177, v130, v131\n\t"
        "v_mfma_f32_16x16x32_bf16 v[120:123], v[48:51], v[28:31], v[120:123]\n\t"
        "v_cvt_pk_bf16_f32 v178, v132, v133\n\t"
        "v_cvt_pk_bf16_f32 v179, v134, v135\n\t"
        "v_mfma_f32_16x16x32_bf16 v[124:127], v[48:51], v[32:35], v[124:127]\n\t"
        "v_permlane16_swap_b32 v160, v162\n\t"
        "v_permlane16_swap_b32 v161, v163\n\t"
        "v_permlane16_swap_b32 v176, v178\n\t"
        "v_permlane16_swap_b32 v177, v179\n\t"
        "s_waitcnt lgkmcnt(0)\n\t"
        "v_cvt_pk_bf16_f32 v164, v104, v105\n\t"
        "v_mfma_f32_32x32x4_2b_bf16 v[0:31], v[188:189], v[176:177], 0\n\t"
        "v_cvt_pk_bf16_f32 v165, v106, v107\n\t"
        "v_cvt_pk_bf16_f32 v166, v108, v109\n\t"
        "v_cvt_pk_bf16_f32 v167, v110, v111\n\t"
        "v_cvt_pk_bf16_f32 v168, v112, v113\n\t"
        "v_cvt_pk_bf16_f32 v169, v114, v115\n\t"
        "v_cvt_pk_bf16_f32 v170, v116, v117\n\t"
        "v_cvt_pk_bf16_f32 v171, v118, v119\n\t"
        "v_permlane16_swap_b32 v164, v166\n\t"
        "v_permlane16_swap_b32 v165, v167\n\t"
        "v_cvt_pk_bf16_f32 v172, v120, v121\n\t"
        "v_cvt_pk_bf16_f32 v173, v122, v123\n\t"
        "v_cvt_pk_bf16_f32 v174, v124, v125\n\t"
        "v_cvt_pk_bf16_f32 v175, v126, v127\n\t"
        "v_permlane16_swap_b32 v168, v170\n\t"
        "v_permlane16_swap_b32 v169, v171\n\t"
        "s_nop 0\n\t"
        "v_permlane16_swap_b32 v172, v174\n\t"
        "v_permlane16_swap_b32 v173, v175\n\t"
        "v_mfma_f32_32x32x16_bf16 v[0:15], v[180:183], v[160:163], v[0:15]\n\t"
        "v_mfma_f32_32x32x16_bf16 v[16:31], v[180:183], v[164:167], v[16:31]\n\t"
        "s_nop 9\n\t"
        "v_mfma_f32_32x32x4_2b_bf16 v[32:63], v[190:191], v[176:177], 0\n\t"
        "v_exp_f32 v0, v0\n\t"
        "v_exp_f32 v1, v1\n\t"
        "v_exp_f32 v2, v2\n\t"
        "v_exp_f32 v3, v3\n\t"
        "v_exp_f32 v4, v4\n\t"
        "v_exp_f32 v5, v5\n\t"
        "v_exp_f32 v6, v6\n\t"
        "v_exp_f32 v7, v7\n\t"
        "v_mfma_f32_32x32x4_2b_bf16 v[96:127], v[222:223], v[222:223], 0\n\t"
        "ds_read2st64_b64 v[192:195], v218 offset0:11 offset1:13\n\t"
        "ds_read_b128 v[200:203], v219 offset:256\n\t"
        "v_exp_f32 v8, v8\n\t"
        "v_exp_f32 v9, v9\n\t"
        "v_exp_f32 v10, v10\n\t"
        "v_exp_f32 v11, v11\n\t"
        "v_exp_f32 v12, v12\n\t"
        "v_exp_f32 v13, v13\n\t"
        "v_exp_f32 v14, v14\n\t"
        "v_exp_f32 v15, v15\n\t"
        "v_exp_f32 v16, v16\n\t"
        "ds_read_b128 v[204:207], v219 offset:1280\n\t"
        "v_mfma_f32_32x32x16_bf16 v[32:47], v[184:187], v[160:163], v[32:47]\n\t"
        "ds_read_b128 v[180:183], v217 offset:7680\n\t"
        "v_exp_f32 v17, v17\n\t"
        "v_exp_f32 v18, v18\n\t"
        "v_exp_f32 v19, v19\n\t"
        "v_mfma_f32_32x32x16_bf16 v[48:63], v[184:187], v[164:167], v[48:63]\n\t"
        "v_exp_f32 v20, v20\n\t"
        "v_exp_f32 v21, v21\n\t"
        "v_exp_f32 v22, v22\n\t"
        "v_exp_f32 v23, v23\n\t"
        "v_mfma_f32_32x32x4_2b_bf16 v[128:159], v[222:223], v[222:223], 0\n\t"
        "v_exp_f32 v24, v24\n\t"
        "v_exp_f32 v25, v25\n\t"
        "v_exp_f32 v26, v26\n\t"
        "v_exp_f32 v27, v27\n\t"
        "v_exp_f32 v28, v28\n\t"
        "v_exp_f32 v29, v29\n\t"
        "v_exp_f32 v30, v30\n\t"
        "v_exp_f32 v31, v31\n\t"
        "s_waitcnt lgkmcnt(1)\n\t"
        "v_mfma_f32_32x32x4_2b_bf16 v[64:95], v[192:193], v[176:177], 0\n\t"
        "v_exp_f32 v32, v32\n\t"
        "v_exp_f32 v33, v33\n\t"
        "v_exp_f32 v34, v34\n\t"
        "v_exp_f32 v35, v35\n\t"
        "v_exp_f32 v36, v36\n\t"
        "v_exp_f32 v37, v37\n\t"
        "v_exp_f32 v38, v38\n\t"
        "v_exp_f32 v39, v39\n\t"
        "v_smfmac_f32_32x32x32_bf16 v[96:111], v[200:203], v[0:7], v216\n\t"
        "s_setprio 2\n\t"
        "ds_read_b128 v[208:211], v219 offset:4096\n\t"
        "ds_read_b128 v[212:215], v219 offset:4864\n\t"
        "v_exp_f32 v40, v40\n\t"
        "v_exp_f32 v41, v41\n\t"
        "v_exp_f32 v42, v42\n\t"
        "v_exp_f32 v43, v43\n\t"
        "v_smfmac_f32_32x32x32_bf16 v[96:111], v[204:207], v[8:15], v216\n\t"
        "s_waitcnt lgkmcnt(2)\n\t"
        "ds_read_b128 v[184:187], v217 offset:8704\n\t"
        "v_exp_f32 v44, v44\n\t"
        "v_exp_f32 v45, v45\n\t"
        "v_exp_f32 v46, v46\n\t"
        "v_exp_f32 v47, v47\n\t"
        "v_mfma_f32_32x32x16_bf16 v[64:79], v[180:183], v[160:163], v[64:79]\n\t"
        "v_exp_f32 v48, v48\n\t"
        "v_exp_f32 v49, v49\n\t"
        "v_exp_f32 v50, v50\n\t"
        "v_exp_f32 v51, v51\n\t"
        "v_mfma_f32_32x32x16_bf16 v[80:95], v[180:183], v[164:167], v[80:95]\n\t"
        "v_exp_f32 v52, v52\n\t"
        "v_exp_f32 v53, v53\n\t"
        "v_exp_f32 v54, v54\n\t"
        "v_exp_f32 v55, v55\n\t"
        "v_smfmac_f32_32x32x32_bf16 v[112:127], v[200:203], v[16:23], v216\n\t"
        "v_mov_b32 v222, %[done]\n\t"
        "v_exp_f32 v56, v56\n\t"
        "v_exp_f32 v57, v57\n\t"
        "v_exp_f32 v58, v58\n\t"
        "v_exp_f32 v59, v59\n\t"
        "v_smfmac_f32_32x32x32_bf16 v[112:127], v[204:207], v[24:31], v216\n\t"
        "s_waitcnt lgkmcnt(1)\n\t"
        "v_exp_f32 v60, v60\n\t"
        "v_exp_f32 v61, v61\n\t"
        "v_exp_f32 v62, v62\n\t"
        "v_exp_f32 v63, v63\n\t"
        "v_mfma_f32_32x32x4_2b_bf16 v[0:31], v[194:195], v[176:177], 0\n\t"
        "v_exp_f32 v64, v64\n\t"
        "v_exp_f32 v65, v65\n\t"
        "v_exp_f32 v66, v66\n\t"
        "v_exp_f32 v67, v67\n\t"
        "v_exp_f32 v68, v68\n\t"
        "v_exp_f32 v69, v69\n\t"
        "v_exp_f32 v70, v70\n\t"
        "v_exp_f32 v71, v71\n\t"
        "v_smfmac_f32_32x32x32_bf16 v[96:111], v[208:211], v[32:39], v216\n\t"
        "ds_read2st64_b64 v[196:199], v218 offset0:22 offset1:24\n\t"
        "ds_read_b128 v[200:203], v219 offset:5888\n\t"
        "v_exp_f32 v72, v72\n\t"
        "v_exp_f32 v73, v73\n\t"
        "v_exp_f32 v74, v74\n\t"
        "v_exp_f32 v75, v75\n\t"
        "v_smfmac_f32_32x32x32_bf16 v[96:111], v[212:215], v[40:47], v216\n\t"
        "s_waitcnt lgkmcnt(2)\n\t"
        "ds_read_b128 v[204:207], v219 offset:6912\n\t"
        "v_exp_f32 v76, v76\n\t"
        "v_exp_f32 v77, v77\n\t"
        "v_exp_f32 v78, v78\n\t"
        "v_exp_f32 v79, v79\n\t"
        "v_mfma_f32_32x32x16_bf16 v[0:15], v[184:187], v[160:163], v[0:15]\n\t"
        "ds_read_b128 v[180:183], v217 offset:13312\n\t"
        "v_exp_f32 v80, v80\n\t"
        "v_exp_f32 v81, v81\n\t"
        "v_exp_f32 v82, v82\n\t"
        "v_exp_f32 v83, v83\n\t"
        "v_mfma_f32_32x32x16_bf16 v[16:31], v[184:187], v[164:167], v[16:31]\n\t"
        "v_exp_f32 v84, v84\n\t"
        "v_exp_f32 v85, v85\n\t"
        "v_exp_f32 v86, v86\n\t"
        "v_exp_f32 v87, v87\n\t"
        "v_smfmac_f32_32x32x32_bf16 v[112:127], v[208:211], v[48:55], v216\n\t"
        "v_exp_f32 v88, v88\n\t"
        "v_exp_f32 v89, v89\n\t"
        "v_exp_f32 v90, v90\n\t"
        "v_exp_f32 v91, v91\n\t"
        "v_smfmac_f32_32x32x32_bf16 v[112:127], v[212:215], v[56:63], v216\n\t"
        "s_waitcnt lgkmcnt(1)\n\t"
        "v_exp_f32 v92, v92\n\t"
        "v_exp_f32 v93, v93\n\t"
        "v_exp_f32 v94, v94\n\t"
        "v_exp_f32 v95, v95\n\t"
        "v_mfma_f32_32x32x4_2b_bf16 v[32:63], v[196:197], v[176:177], 0\n\t"
        "v_exp_f32 v0, v0\n\t"
        "v_exp_f32 v1, v1\n\t"
        "v_exp_f32 v2, v2\n\t"
        "v_exp_f32 v3, v3\n\t"
        "v_exp_f32 v4, v4\n\t"
        "v_exp_f32 v5, v5\n\t"
        "v_exp_f32 v6, v6\n\t"
        "v_exp_f32 v7, v7\n\t"
        "v_smfmac_f32_32x32x32_bf16 v[96:111], v[200:203], v[64:71], v216\n\t"
        "ds_read_b128 v[208:211], v219 offset:9728\n\t"
        "ds_read_b128 v[212:215], v219 offset:10496\n\t"
        "v_exp_f32 v8, v8\n\t"
        "v_exp_f32 v9, v9\n\t"
        "v_exp_f32 v10, v10\n\t"
        "v_exp_f32 v11, v11\n\t"
        "v_smfmac_f32_32x32x32_bf16 v[96:111], v[204:207], v[72:79], v216\n\t"
        "s_waitcnt lgkmcnt(2)\n\t"
        "ds_read_b128 v[184:187], v217 offset:14336\n\t"
        "v_exp_f32 v12, v12\n\t"
        "v_exp_f32 v13, v13\n\t"
        "v_exp_f32 v14, v14\n\t"
        "v_exp_f32 v15, v15\n\t"
        "v_mfma_f32_32x32x16_bf16 v[32:47], v[180:183], v[160:163], v[32:47]\n\t"
        "v_exp_f32 v16, v16\n\t"
        "v_exp_f32 v17, v17\n\t"
        "v_exp_f32 v18, v18\n\t"
        "v_exp_f32 v19, v19\n\t"
        "v_mfma_f32_32x32x16_bf16 v[48:63], v[180:183], v[164:167], v[48:63]\n\t"
        "v_exp_f32 v20, v20\n\t"
        "v_exp_f32 v21, v21\n\t"
        "v_exp_f32 v22, v22\n\t"
        "v_exp_f32 v23, v23\n\t"
        "v_smfmac_f32_32x32x32_bf16 v[112:127], v[200:203], v[80:87], v216\n\t"
        "v_exp_f32 v24, v24\n\t"
        "v_exp_f32 v25, v25\n\t"
        "v_exp_f32 v26, v26\n\t"
        "v_exp_f32 v27, v27\n\t"
        "v_smfmac_f32_32x32x32_bf16 v[112:127], v[204:207], v[88:95], v216\n\t"
        "s_waitcnt lgkmcnt(1)\n\t"
        "v_exp_f32 v28, v28\n\t"
        "v_exp_f32 v29, v29\n\t"
        "v_exp_f32 v30, v30\n\t"
        "v_exp_f32 v31, v31\n\t"
        "v_mfma_f32_32x32x4_2b_bf16 v[64:95], v[198:199], v[176:177], 0\n\t"
        "v_exp_f32 v32, v32\n\t"
        "v_exp_f32 v33, v33\n\t"
        "v_exp_f32 v34, v34\n\t"
        "v_exp_f32 v35, v35\n\t"
        "v_exp_f32 v36, v36\n\t"
        "v_exp_f32 v37, v37\n\t"
        "v_exp_f32 v38, v38\n\t"
        "v_exp_f32 v39, v39\n\t"
        "v_smfmac_f32_32x32x32_bf16 v[96:111], v[208:211], v[0:7], v216\n\t"
        "ds_read2st64_b64 v[188:191], v218 offset0:33 offset1:35\n\t"
        "ds_read_b128 v[200:203], v219 offset:11520\n\t"
        "v_exp_f32 v40, v40\n\t"
        "v_exp_f32 v41, v41\n\t"
        "v_exp_f32 v42, v42\n\t"
        "v_exp_f32 v43, v43\n\t"
        "v_smfmac_f32_32x32x32_bf16 v[96:111], v[212:215], v[8:15], v216\n\t"
        "s_waitcnt lgkmcnt(2)\n\t"
        "ds_read_b128 v[204:207], v219 offset:12544\n\t"
        "v_exp_f32 v44, v44\n\t"
        "v_exp_f32 v45, v45\n\t"
        "v_exp_f32 v46, v46\n\t"
        "v_exp_f32 v47, v47\n\t"
        "v_mfma_f32_32x32x16_bf16 v[64:79], v[184:187], v[160:163], v[64:79]\n\t"
        "ds_read_b128 v[180:183], v217 offset:18944\n\t"
        "v_exp_f32 v48, v48\n\t"
        "v_exp_f32 v49, v49\n\t"
        "v_exp_f32 v50, v50\n\t"
        "v_exp_f32 v51, v51\n\t"
        "v_mfma_f32_32x32x16_bf16 v[80:95], v[184:187], v[164:167], v[80:95]\n\t"
        "v_exp_f32 v52, v52\n\t"
        "v_exp_f32 v53, v53\n\t"
        "v_exp_f32 v54, v54\n\t"
        "v_exp_f32 v55, v55\n\t"
        "v_smfmac_f32_32x32x32_bf16 v[112:127], v[208:211], v[16:23], v216\n\t"
        "v_exp_f32 v56, v56\n\t"
        "v_exp_f32 v57, v57\n\t"
        "v_exp_f32 v58, v58\n\t"
        "v_exp_f32 v59, v59\n\t"
        "v_smfmac_f32_32x32x32_bf16 v[112:127], v[212:215], v[24:31], v216\n\t"
        "s_waitcnt lgkmcnt(1)\n\t"
        "v_exp_f32 v60, v60\n\t"
        "v_exp_f32 v61, v61\n\t"
        "v_exp_f32 v62, v62\n\t"
        "v_exp_f32 v63, v63\n\t"
        "v_mfma_f32_32x32x4_2b_bf16 v[0:31], v[188:189], v[176:177], 0\n\t"
        "v_exp_f32 v64, v64\n\t"
        "v_exp_f32 v65, v65\n\t"
        "v_exp_f32 v66, v66\n\t"
        "v_exp_f32 v67, v67\n\t"
        "v_exp_f32 v68, v68\n\t"
        "v_exp_f32 v69, v69\n\t"
        "v_exp_f32 v70, v70\n\t"
        "v_exp_f32 v71, v71\n\t"
        "v_smfmac_f32_32x32x32_bf16 v[96:111], v[200:203], v[32:39], v216\n\t"
        "ds_read_b32 v229, v231\n\t"
        "ds_read_b128 v[208:211], v219 offset:15360\n\t"
        "ds_read_b128 v[212:215], v219 offset:16128\n\t"
        "v_exp_f32 v72, v72\n\t"
        "v_exp_f32 v73, v73\n\t"
        "v_exp_f32 v74, v74\n\t"
        "v_exp_f32 v75, v75\n\t"
        "v_smfmac_f32_32x32x32_bf16 v[96:111], v[204:207], v[40:47], v216\n\t"
        "s_waitcnt lgkmcnt(3)\n\t"
        "ds_read_b128 v[184:187], v217 offset:19968\n\t"
        "v_exp_f32 v76, v76\n\t"
        "v_exp_f32 v77, v77\n\t"
        "v_exp_f32 v78, v78\n\t"
        "v_exp_f32 v79, v79\n\t"
        "v_mfma_f32_32x32x16_bf16 v[0:15], v[180:183], v[160:163], v[0:15]\n\t"
        "v_exp_f32 v80, v80\n\t"
        "v_exp_f32 v81, v81\n\t"
        "v_exp_f32 v82, v82\n\t"
        "v_exp_f32 v83, v83\n\t"
        "v_mfma_f32_32x32x16_bf16 v[16:31], v[180:183], v[164:167], v[16:31]\n\t"
        "v_exp_f32 v84, v84\n\t"
        "v_exp_f32 v85, v85\n\t"
        "v_exp_f32 v86, v86\n\t"
        "v_exp_f32 v87, v87\n\t"
        "v_smfmac_f32_32x32x32_bf16 v[112:127], v[200:203], v[48:55], v216\n\t"
        "v_exp_f32 v88, v88\n\t"
        "v_exp_f32 v89, v89\n\t"
        "v_exp_f32 v90, v90\n\t"
        "v_exp_f32 v91, v91\n\t"
        "v_smfmac_f32_32x32x32_bf16 v[112:127], v[204:207], v[56:63], v216\n\t"
        "s_waitcnt lgkmcnt(1)\n\t"
        "v_cmp_gt_u32 vcc, 1, v229\n\t"
        "v_exp_f32 v92, v92\n\t"
        "v_exp_f32 v93, v93\n\t"
        "v_exp_f32 v94, v94\n\t"
        "v_exp_f32 v95, v95\n\t"
        "v_mfma_f32_32x32x4_2b_bf16 v[32:63], v[190:191], v[176:177], 0\n\t"
        "v_exp_f32 v0, v0\n\t"
        "v_exp_f32 v1, v1\n\t"
        "v_exp_f32 v2, v2\n\t"
        "v_exp_f32 v3, v3\n\t"
        "v_exp_f32 v4, v4\n\t"
        "v_exp_f32 v5, v5\n\t"
        "v_exp_f32 v6, v6\n\t"
        "v_exp_f32 v7, v7\n\t"
        "v_smfmac_f32_32x32x32_bf16 v[96:111], v[208:211], v[64:71], v216\n\t"
        "s_cbranch_vccnz 30b\n\t"
        "31:\n\t"
        "ds_read2st64_b64 v[192:195], v218 offset0:44 offset1:46\n\t"
        "ds_read_b128 v[200:203], v219 offset:17152\n\t"
        "v_exp_f32 v8, v8\n\t"
        "v_exp_f32 v9, v9\n\t"
        "v_exp_f32 v10, v10\n\t"
        "v_exp_f32 v11, v11\n\t"
        "v_smfmac_f32_32x32x32_bf16 v[96:111], v[212:215], v[72:79], v216\n\t"
        "s_waitcnt lgkmcnt(2)\n\t"
        "ds_read_b128 v[204:207], v219 offset:18176\n\t"
        "v_exp_f32 v12, v12\n\t"
        "v_exp_f32 v13, v13\n\t"
        "v_exp_f32 v14, v14\n\t"
        "v_exp_f32 v15, v15\n\t"
        "v_mfma_f32_32x32x16_bf16 v[32:47], v[184:187], v[160:163], v[32:47]\n\t"
        "ds_read_b128 v[180:183], v217 offset:24576\n\t"
        "v_exp_f32 v16, v16\n\t"
        "v_exp_f32 v17, v17\n\t"
        "v_exp_f32 v18, v18\n\t"
        "v_exp_f32 v19, v19\n\t"
        "v_mfma_f32_32x32x16_bf16 v[48:63], v[184:187], v[164:167], v[48:63]\n\t"
        "v_exp_f32 v20, v20\n\t"
        "v_exp_f32 v21, v21\n\t"
        "v_exp_f32 v22, v22\n\t"
        "v_exp_f32 v23, v23\n\t"
        "v_smfmac_f32_32x32x32_bf16 v[112:127], v[208:211], v[80:87], v216\n\t"
        "buffer_load_dword v220, v220, %[srdws], 0 offen sc1\n\t"
        "v_exp_f32 v24, v24\n\t"
        "v_exp_f32 v25, v25\n\t"
        "v_exp_f32 v26, v26\n\t"
        "v_exp_f32 v27, v27\n\t"
        "v_smfmac_f32_32x32x32_bf16 v[112:127], v[212:215], v[88:95], v216\n\t"
        "s_waitcnt lgkmcnt(1)\n\t"
        "v_exp_f32 v28, v28\n\t"
        "v_exp_f32 v29, v29\n\t"
        "v_exp_f32 v30, v30\n\t"
        "v_exp_f32 v31, v31\n\t"
        "v_mfma_f32_32x32x4_2b_bf16 v[64:95], v[192:193], v[176:177], 0\n\t"
        "v_exp_f32 v32, v32\n\t"
        "v_exp_f32 v33, v33\n\t"
        "v_exp_f32 v34, v34\n\t"
        "v_exp_f32 v35, v35\n\t"
        "v_exp_f32 v36, v36\n\t"
        "v_exp_f32 v37, v37\n\t"
        "v_exp_f32 v38, v38\n\t"
        "v_exp_f32 v39, v39\n\t"
        "v_smfmac_f32_32x32x32_bf16 v[96:111], v[200:203], v[0:7], v216\n\t"
        "ds_read_b128 v[208:211], v219 offset:20992\n\t"
        "ds_read_b128 v[212:215], v219 offset:21760\n\t"
        "v_exp_f32 v40, v40\n\t"
        "v_exp_f32 v41, v41\n\t"
        "v_exp_f32 v42, v42\n\t"
        "v_exp_f32 v43, v43\n\t"
        "v_smfmac_f32_32x32x32_bf16 v[96:111], v[204:207], v[8:15], v216\n\t"
        "s_waitcnt lgkmcnt(2)\n\t"
        "ds_read_b128 v[184:187], v217 offset:25600\n\t"
        "v_exp_f32 v44, v44\n\t"
        "v_exp_f32 v45, v45\n\t"
        "v_exp_f32 v46, v46\n\t"
        "v_exp_f32 v47, v47\n\t"
        "v_mfma_f32_32x32x16_bf16 v[64:79], v[180:183], v[160:163], v[64:79]\n\t"
        "v_exp_f32 v48, v48\n\t"
        "v_exp_f32 v49, v49\n\t"
        "v_exp_f32 v50, v50\n\t"
        "v_exp_f32 v51, v51\n\t"
        "v_mfma_f32_32x32x16_bf16 v[80:95], v[180:183], v[164:167], v[80:95]\n\t"
        "v_exp_f32 v52, v52\n\t"
        "v_exp_f32 v53, v53\n\t"
        "v_exp_f32 v54, v54\n\t"
        "v_exp_f32 v55, v55\n\t"
        "v_smfmac_f32_32x32x32_bf16 v[112:127], v[200:203], v[16:23], v216\n\t"
        "v_exp_f32 v56, v56\n\t"
        "v_exp_f32 v57, v57\n\t"
        "v_exp_f32 v58, v58\n\t"
        "v_exp_f32 v59, v59\n\t"
        "v_smfmac_f32_32x32x32_bf16 v[112:127], v[204:207], v[24:31], v216\n\t"
        "s_waitcnt lgkmcnt(1)\n\t"
        "v_exp_f32 v60, v60\n\t"
        "v_exp_f32 v61, v61\n\t"
        "v_exp_f32 v62, v62\n\t"
        "v_exp_f32 v63, v63\n\t"
        "v_mfma_f32_32x32x4_2b_bf16 v[0:31], v[194:195], v[176:177], 0\n\t"
        "v_exp_f32 v64, v64\n\t"
        "v_exp_f32 v65, v65\n\t"
        "v_exp_f32 v66, v66\n\t"
        "v_exp_f32 v67, v67\n\t"
        "v_exp_f32 v68, v68\n\t"
        "v_exp_f32 v69, v69\n\t"
        "v_exp_f32 v70, v70\n\t"
        "v_exp_f32 v71, v71\n\t"
        "v_smfmac_f32_32x32x32_bf16 v[96:111], v[208:211], v[32:39], v216\n\t"
        "ds_read2st64_b64 v[196:199], v218 offset0:55 offset1:57\n\t"
        "ds_read_b128 v[200:203], v219 offset:22784\n\t"
        "v_exp_f32 v72, v72\n\t"
        "v_exp_f32 v73, v73\n\t"
        "v_exp_f32 v74, v74\n\t"
        "v_exp_f32 v75, v75\n\t"
        "v_smfmac_f32_32x32x32_bf16 v[96:111], v[212:215], v[40:47], v216\n\t"
        "s_waitcnt lgkmcnt(2)\n\t"
        "ds_read_b128 v[204:207], v219 offset:23808\n\t"
        "v_exp_f32 v76, v76\n\t"
        "v_exp_f32 v77, v77\n\t"
        "v_exp_f32 v78, v78\n\t"
        "v_exp_f32 v79, v79\n\t"
        "v_mfma_f32_32x32x16_bf16 v[0:15], v[184:187], v[160:163], v[0:15]\n\t"
        "ds_read_b128 v[180:183], v217 offset:30208\n\t"
        "v_exp_f32 v80, v80\n\t"
        "v_exp_f32 v81, v81\n\t"
        "v_exp_f32 v82, v82\n\t"
        "v_exp_f32 v83, v83\n\t"
        "v_mfma_f32_32x32x16_bf16 v[16:31], v[184:187], v[164:167], v[16:31]\n\t"
        "v_exp_f32 v84, v84\n\t"
        "v_exp_f32 v85, v85\n\t"
        "v_exp_f32 v86, v86\n\t"
        "v_exp_f32 v87, v87\n\t"
        "v_smfmac_f32_32x32x32_bf16 v[112:127], v[208:211], v[48:55], v216\n\t"
        "v_exp_f32 v88, v88\n\t"
        "v_exp_f32 v89, v89\n\t"
        "v_exp_f32 v90, v90\n\t"
        "v_exp_f32 v91, v91\n\t"
        "v_smfmac_f32_32x32x32_bf16 v[112:127], v[212:215], v[56:63], v216\n\t"
        "s_waitcnt lgkmcnt(1)\n\t"
        "v_exp_f32 v92, v92\n\t"
        "v_exp_f32 v93, v93\n\t"
        "v_exp_f32 v94, v94\n\t"
        "v_exp_f32 v95, v95\n\t"
        "v_mfma_f32_32x32x4_2b_bf16 v[32:63], v[196:197], v[176:177], 0\n\t"
        "v_exp_f32 v0, v0\n\t"
        "v_exp_f32 v1, v1\n\t"
        "v_exp_f32 v2, v2\n\t"
        "v_exp_f32 v3, v3\n\t"
        "v_exp_f32 v4, v4\n\t"
        "v_exp_f32 v5, v5\n\t"
        "v_exp_f32 v6, v6\n\t"
        "v_exp_f32 v7, v7\n\t"
        "v_smfmac_f32_32x32x32_bf16 v[96:111], v[200:203], v[64:71], v216\n\t"
        "ds_read_b128 v[208:211], v219 offset:26624\n\t"
        "ds_read_b128 v[212:215], v219 offset:27392\n\t"
        "v_exp_f32 v8, v8\n\t"
        "v_exp_f32 v9, v9\n\t"
        "v_exp_f32 v10, v10\n\t"
        "v_exp_f32 v11, v11\n\t"
        "v_smfmac_f32_32x32x32_bf16 v[96:111], v[204:207], v[72:79], v216\n\t"
        "s_waitcnt lgkmcnt(2)\n\t"
        "ds_read_b128 v[184:187], v217 offset:31232\n\t"
        "v_exp_f32 v12, v12\n\t"
        "v_exp_f32 v13, v13\n\t"
        "v_exp_f32 v14, v14\n\t"
        "v_exp_f32 v15, v15\n\t"
        "v_mfma_f32_32x32x16_bf16 v[32:47], v[180:183], v[160:163], v[32:47]\n\t"
        "v_exp_f32 v16, v16\n\t"
        "v_exp_f32 v17, v17\n\t"
        "v_exp_f32 v18, v18\n\t"
        "v_exp_f32 v19, v19\n\t"
        "v_mfma_f32_32x32x16_bf16 v[48:63], v[180:183], v[164:167], v[48:63]\n\t"
        "v_exp_f32 v20, v20\n\t"
        "v_exp_f32 v21, v21\n\t"
        "v_exp_f32 v22, v22\n\t"
        "v_exp_f32 v23, v23\n\t"
        "v_smfmac_f32_32x32x32_bf16 v[112:127], v[200:203], v[80:87], v216\n\t"
        "v_exp_f32 v24, v24\n\t"
        "v_exp_f32 v25, v25\n\t"
        "v_exp_f32 v26, v26\n\t"
        "v_exp_f32 v27, v27\n\t"
        "v_smfmac_f32_32x32x32_bf16 v[112:127], v[204:207], v[88:95], v216\n\t"
        "s_waitcnt lgkmcnt(1)\n\t"
        "v_exp_f32 v28, v28\n\t"
        "v_exp_f32 v29, v29\n\t"
        "v_exp_f32 v30, v30\n\t"
        "v_exp_f32 v31, v31\n\t"
        "v_mfma_f32_32x32x4_2b_bf16 v[64:95], v[198:199], v[176:177], 0\n\t"
        "v_exp_f32 v32, v32\n\t"
        "v_exp_f32 v33, v33\n\t"
        "v_exp_f32 v34, v34\n\t"
        "v_exp_f32 v35, v35\n\t"
        "v_exp_f32 v36, v36\n\t"
        "v_exp_f32 v37, v37\n\t"
        "v_exp_f32 v38, v38\n\t"
        "v_exp_f32 v39, v39\n\t"
        "v_smfmac_f32_32x32x32_bf16 v[96:111], v[208:211], v[0:7], v216\n\t"
        "ds_read2st64_b64 v[188:191], v218 offset0:66 offset1:68\n\t"
        "ds_read_b128 v[200:203], v219 offset:28416\n\t"
        "v_exp_f32 v40, v40\n\t"
        "v_exp_f32 v41, v41\n\t"
        "v_exp_f32 v42, v42\n\t"
        "v_exp_f32 v43, v43\n\t"
        "v_smfmac_f32_32x32x32_bf16 v[96:111], v[212:215], v[8:15], v216\n\t"
        "s_waitcnt lgkmcnt(2)\n\t"
        "ds_read_b128 v[204:207], v219 offset:29440\n\t"
        "v_exp_f32 v44, v44\n\t"
        "v_exp_f32 v45, v45\n\t"
        "v_exp_f32 v46, v46\n\t"
        "v_exp_f32 v47, v47\n\t"
        "v_mfma_f32_32x32x16_bf16 v[64:79], v[184:187], v[160:163], v[64:79]\n\t"
        "ds_read_b128 v[180:183], v217 offset:35840\n\t"
        "v_exp_f32 v48, v48\n\t"
        "v_exp_f32 v49, v49\n\t"
        "v_exp_f32 v50, v50\n\t"
        "v_exp_f32 v51, v51\n\t"
        "v_mfma_f32_32x32x16_bf16 v[80:95], v[184:187], v[164:167], v[80:95]\n\t"
        "v_exp_f32 v52, v52\n\t"
        "v_exp_f32 v53, v53\n\t"
        "v_exp_f32 v54, v54\n\t"
        "v_exp_f32 v55, v55\n\t"
        "v_smfmac_f32_32x32x32_bf16 v[112:127], v[208:211], v[16:23], v216\n\t"
        "v_exp_f32 v56, v56\n\t"
        "v_exp_f32 v57, v57\n\t"
        "v_exp_f32 v58, v58\n\t"
        "v_exp_f32 v59, v59\n\t"
        "v_smfmac_f32_32x32x32_bf16 v[112:127], v[212:215], v[24:31], v216\n\t"
        "s_waitcnt lgkmcnt(1)\n\t"
        "v_exp_f32 v60, v60\n\t"
        "v_exp_f32 v61, v61\n\t"
        "v_exp_f32 v62, v62\n\t"
        "v_exp_f32 v63, v63\n\t"
        "v_mfma_f32_32x32x4_2b_bf16 v[0:31], v[188:189], v[176:177], 0\n\t"
        "v_exp_f32 v64, v64\n\t"
        "v_exp_f32 v65, v65\n\t"
        "v_exp_f32 v66, v66\n\t"
        "v_exp_f32 v67, v67\n\t"
        "v_exp_f32 v68, v68\n\t"
        "v_exp_f32 v69, v69\n\t"
        "v_exp_f32 v70, v70\n\t"
        "v_exp_f32 v71, v71\n\t"
        "v_smfmac_f32_32x32x32_bf16 v[96:111], v[200:203], v[32:39], v216\n\t"
        "ds_read_b128 v[208:211], v219 offset:32256\n\t"
        "v_exp_f32 v72, v72\n\t"
        "v_exp_f32 v73, v73\n\t"
        "v_exp_f32 v74, v74\n\t"
        "v_exp_f32 v75, v75\n\t"
        "v_smfmac_f32_32x32x32_bf16 v[96:111], v[204:207], v[40:47], v216\n\t"
        "s_waitcnt lgkmcnt(1)\n\t"
        "ds_read_b128 v[212:215], v219 offset:33024\n\t"
        "v_exp_f32 v76, v76\n\t"
        "v_exp_f32 v77, v77\n\t"
        "v_exp_f32 v78, v78\n\t"
        "v_exp_f32 v79, v79\n\t"
        "v_mfma_f32_32x32x16_bf16 v[0:15], v[180:183], v[160:163], v[0:15]\n\t"
        "v_exp_f32 v80, v80\n\t"
        "v_exp_f32 v81, v81\n\t"
        "v_exp_f32 v82, v82\n\t"
        "v_exp_f32 v83, v83\n\t"
        "v_mfma_f32_32x32x16_bf16 v[16:31], v[180:183], v[164:167], v[16:31]\n\t"
        "v_exp_f32 v84, v84\n\t"
        "v_exp_f32 v85, v85\n\t"
        "v_exp_f32 v86, v86\n\t"
        "v_exp_f32 v87, v87\n\t"
        "v_smfmac_f32_32x32x32_bf16 v[112:127], v[200:203], v[48:55], v216\n\t"
        "v_exp_f32 v88, v88\n\t"
        "v_exp_f32 v89, v89\n\t"
        "v_exp_f32 v90, v90\n\t"
        "v_exp_f32 v91, v91\n\t"
        "v_smfmac_f32_32x32x32_bf16 v[112:127], v[204:207], v[56:63], v216\n\t"
        "s_waitcnt lgkmcnt(0)\n\t"
        "v_exp_f32 v92, v92\n\t"
        "v_exp_f32 v93, v93\n\t"
        "v_exp_f32 v94, v94\n\t"
        "v_exp_f32 v95, v95\n\t"
        "v_mfma_f32_32x32x4_2b_bf16 v[32:63], v[188:189], v[178:179], 0\n\t"
        "v_exp_f32 v0, v0\n\t"
        "v_exp_f32 v1, v1\n\t"
        "v_exp_f32 v2, v2\n\t"
        "v_exp_f32 v3, v3\n\t"
        "v_exp_f32 v4, v4\n\t"
        "v_exp_f32 v5, v5\n\t"
        "v_exp_f32 v6, v6\n\t"
        "v_exp_f32 v7, v7\n\t"
        "v_smfmac_f32_32x32x32_bf16 v[96:111], v[208:211], v[64:71], v216\n\t"
        "ds_read_b32 v229, v231\n\t"
        "ds_read_b128 v[200:203], v219 offset:34048\n\t"
        "ds_read_b128 v[204:207], v219 offset:35072\n\t"
        "v_exp_f32 v8, v8\n\t"
        "v_exp_f32 v9, v9\n\t"
        "v_exp_f32 v10, v10\n\t"
        "v_exp_f32 v11, v11\n\t"
        "v_smfmac_f32_32x32x32_bf16 v[96:111], v[212:215], v[72:79], v216\n\t"
        "ds_read_b128 v[184:187], v217 offset:36864\n\t"
        "v_exp_f32 v12, v12\n\t"
        "v_exp_f32 v13, v13\n\t"
        "v_exp_f32 v14, v14\n\t"
        "v_exp_f32 v15, v15\n\t"
        "v_mfma_f32_32x32x16_bf16 v[32:47], v[180:183], v[168:171], v[32:47]\n\t"
        "s_mov_b32 %[xp], 2\n\t"
        "v_exp_f32 v16, v16\n\t"
        "v_exp_f32 v17, v17\n\t"
        "v_exp_f32 v18, v18\n\t"
        "v_exp_f32 v19, v19\n\t"
        "v_mfma_f32_32x32x16_bf16 v[48:63], v[180:183], v[172:175], v[48:63]\n\t"
        "v_exp_f32 v20, v20\n\t"
        "v_exp_f32 v21, v21\n\t"
        "v_exp_f32 v22, v22\n\t"
        "v_exp_f32 v23, v23\n\t"
        "v_smfmac_f32_32x32x32_bf16 v[112:127], v[208:211], v[80:87], v216\n\t"
        "v_add_u32 v217, 0xfffeb600, v217\n\t"
        "v_add_u32 v218, 0xfffeb600, v218\n\t"
        "v_add_u32 v219, 0xfffeb600, v219\n\t"
        "v_exp_f32 v24, v24\n\t"
        "v_exp_f32 v25, v25\n\t"
        "v_exp_f32 v26, v26\n\t"
        "v_exp_f32 v27, v27\n\t"
        "v_smfmac_f32_32x32x32_bf16 v[112:127], v[212:215], v[88:95], v216\n\t"
        "s_waitcnt lgkmcnt(1)\n\t"
        "v_cmp_gt_u32 vcc, %[xp], v229\n\t"
        "v_exp_f32 v28, v28\n\t"
        "v_exp_f32 v29, v29\n\t"
        "v_exp_f32 v30, v30\n\t"
        "v_exp_f32 v31, v31\n\t"
        "s_cbranch_vccnz 32b\n\t"
        "1:\n\t"
        "v_mfma_f32_32x32x4_2b_bf16 v[64:95], v[190:191], v[176:177], 0\n\t"
        "v_exp_f32 v32, v32\n\t"
        "v_exp_f32 v33, v33\n\t"
        "v_exp_f32 v34, v34\n\t"
        "v_exp_f32 v35, v35\n\t"
        "v_exp_f32 v36, v36\n\t"
        "v_exp_f32 v37, v37\n\t"
        "v_exp_f32 v38, v38\n\t"
        "v_exp_f32 v39, v39\n\t"
        "v_exp_f32 v40, v40\n\t"
        "v_exp_f32 v41, v41\n\t"
        "v_smfmac_f32_32x32x32_bf16 v[96:111], v[200:203], v[0:7], v216\n\t"
        "ds_read2st64_b64 v[192:195], v218 offset0:3 offset1:5\n\t"
        "ds_read_b128 v[208:211], v219 offset:0\n\t"
        "v_exp_f32 v42, v42\n\t"
        "v_exp_f32 v43, v43\n\t"
        "v_exp_f32 v44, v44\n\t"
        "v_smfmac_f32_32x32x32_bf16 v[96:111], v[204:207], v[8:15], v216\n\t"
        "s_waitcnt lgkmcnt(2)\n\t"
        "ds_read_b128 v[212:215], v219 offset:768\n\t"
        "v_exp_f32 v45, v45\n\t"
        "v_exp_f32 v46, v46\n\t"
        "v_exp_f32 v47, v47\n\t"
        "v_mfma_f32_32x32x16_bf16 v[64:79], v[184:187], v[160:163], v[64:79]\n\t"
        "v_exp_f32 v48, v48\n\t"
        "v_exp_f32 v49, v49\n\t"
        "v_exp_f32 v50, v50\n\t"
        "v_mfma_f32_32x32x16_bf16 v[80:95], v[184:187], v[164:167], v[80:95]\n\t"
        "v_exp_f32 v51, v51\n\t"
        "v_exp_f32 v52, v52\n\t"
        "v_exp_f32 v53, v53\n\t"
        "v_smfmac_f32_32x32x32_bf16 v[112:127], v[200:203], v[16:23], v216\n\t"
        "ds_write_b32 v222, v223\n\t"
        "v_exp_f32 v54, v54\n\t"
        "v_exp_f32 v55, v55\n\t"
        "v_exp_f32 v56, v56\n\t"
        "v_smfmac_f32_32x32x32_bf16 v[112:127], v[204:207], v[24:31], v216\n\t"
        "v_exp_f32 v57, v57\n\t"
        "v_exp_f32 v58, v58\n\t"
        "v_exp_f32 v59, v59\n\t"
        "v_exp_f32 v60, v60\n\t"
        "v_exp_f32 v61, v61\n\t"
        "v_exp_f32 v62, v62\n\t"
        "v_exp_f32 v63, v63\n\t"
        "v_mfma_f32_32x32x4_2b_bf16 v[0:31], v[190:191], v[178:179], 0\n\t"
        "v_exp_f32 v64, v64\n\t"
        "v_exp_f32 v65, v65\n\t"
        "v_exp_f32 v66, v66\n\t"
        "v_exp_f32 v67, v67\n\t"
        "v_exp_f32 v68, v68\n\t"
        "v_exp_f32 v69, v69\n\t"
        "v_exp_f32 v70, v70\n\t"
        "v_exp_f32 v71, v71\n\t"
        "v_exp_f32 v72, v72\n\t"
        "v_exp_f32 v73, v73\n\t"
        "v_smfmac_f32_32x32x32_bf16 v[128:143], v[200:203], v[32:39], v216\n\t"
        "ds_read_b128 v[180:183], v217 offset:3584\n\t"
        "v_exp_f32 v74, v74\n\t"
        "v_exp_f32 v75, v75\n\t"
        "v_exp_f32 v76, v76\n\t"
        "v_smfmac_f32_32x32x32_bf16 v[128:143], v[204:207], v[40:47], v216\n\t"
        "v_exp_f32 v77, v77\n\t"
        "v_exp_f32 v78, v78\n\t"
        "v_exp_f32 v79, v79\n\t"
        "v_mfma_f32_32x32x16_bf16 v[0:15], v[184:187], v[168:171], v[0:15]\n\t"
        "v_exp_f32 v80, v80\n\t"
        "v_exp_f32 v81, v81\n\t"
        "v_exp_f32 v82, v82\n\t"
        "v_mfma_f32_32x32x16_bf16 v[16:31], v[184:187], v[172:175], v[16:31]\n\t"
        "v_exp_f32 v83, v83\n\t"
        "v_exp_f32 v84, v84\n\t"
        "v_exp_f32 v85, v85\n\t"
        "v_smfmac_f32_32x32x32_bf16 v[144:159], v[200:203], v[48:55], v216\n\t"
        "s_add_u32 %[it], %[it], 1\n\t"
        "s_cmp_eq_u32 %[it], 12\n\t"
        "v_add_u32 v223, 1, v223\n\t"
        "v_exp_f32 v86, v86\n\t"
        "v_exp_f32 v87, v87\n\t"
        "v_exp_f32 v88, v88\n\t"
        "v_smfmac_f32_32x32x32_bf16 v[144:159], v[204:207], v[56:63], v216\n\t"
        "s_waitcnt lgkmcnt(1)\n\t"
        "s_cselect_b32 %[t0], 0xffff7c00, %[chsz]\n\t"
        "v_exp_f32 v89, v89\n\t"
        "v_exp_f32 v90, v90\n\t"
        "v_exp_f32 v91, v91\n\t"
        "v_exp_f32 v92, v92\n\t"
        "v_exp_f32 v93, v93\n\t"
        "v_exp_f32 v94, v94\n\t"
        "v_exp_f32 v95, v95\n\t"
        "v_mfma_f32_32x32x4_2b_bf16 v[32:63], v[192:193], v[176:177], 0\n\t"
        "v_exp_f32 v0, v0\n\t"
        "v_exp_f32 v1, v1\n\t"
        "v_exp_f32 v2, v2\n\t"
        "v_exp_f32 v3, v3\n\t"
        "v_exp_f32 v4, v4\n\t"
        "v_exp_f32 v5, v5\n\t"
        "v_exp_f32 v6, v6\n\t"
        "v_exp_f32 v7, v7\n\t"
        "v_exp_f32 v8, v8\n\t"
        "v_exp_f32 v9, v9\n\t"
        "v_smfmac_f32_32x32x32_bf16 v[96:111], v[208:211], v[64:71], v216\n\t"
        "ds_read_b128 v[200:203], v219 offset:1792\n\t"
        "v_exp_f32 v10, v10\n\t"
        "v_exp_f32 v11, v11\n\t"
        "v_exp_f32 v12, v12\n\t"
        "v_smfmac_f32_32x32x32_bf16 v[96:111], v[212:215], v[72:79], v216\n\t"
        "s_waitcnt lgkmcnt(1)\n\t"
        "ds_read_b128 v[204:207], v219 offset:2816\n\t"
        "v_exp_f32 v13, v13\n\t"
        "v_exp_f32 v14, v14\n\t"
        "v_exp_f32 v15, v15\n\t"
        "v_mfma_f32_32x32x16_bf16 v[32:47], v[180:183], v[160:163], v[32:47]\n\t"
        "v_exp_f32 v16, v16\n\t"
        "v_exp_f32 v17, v17\n\t"
        "v_exp_f32 v18, v18\n\t"
        "v_mfma_f32_32x32x16_bf16 v[48:63], v[180:183], v[164:167], v[48:63]\n\t"
        "v_exp_f32 v19, v19\n\t"
        "v_exp_f32 v20, v20\n\t"
        "v_exp_f32 v21, v21\n\t"
        "v_smfmac_f32_32x32x32_bf16 v[112:127], v[208:211], v[80:87], v216\n\t"
        "s_sub_u32 %[xp], %[it], 9\n\t"
        "v_exp_f32 v22, v22\n\t"
        "v_exp_f32 v23, v23\n\t"
        "v_exp_f32 v24, v24\n\t"
        "v_smfmac_f32_32x32x32_bf16 v[112:127], v[212:215], v[88:95], v216\n\t"
        "v_exp_f32 v25, v25\n\t"
        "v_exp_f32 v26, v26\n\t"
        "v_exp_f32 v27, v27\n\t"
        "v_exp_f32 v28, v28\n\t"
        "v_exp_f32 v29, v29\n\t"
        "v_exp_f32 v30, v30\n\t"
        "v_exp_f32 v31, v31\n\t"
        "v_mfma_f32_32x32x4_2b_bf16 v[64:95], v[192:193], v[178:179], 0\n\t"
        "v_exp_f32 v32, v32\n\t"
        "v_exp_f32 v33, v33\n\t"
        "v_exp_f32 v34, v34\n\t"
        "v_exp_f32 v35, v35\n\t"
        "v_exp_f32 v36, v36\n\t"
        "v_exp_f32 v37, v37\n\t"
        "v_exp_f32 v38, v38\n\t"
        "v_exp_f32 v39, v39\n\t"
        "v_exp_f32 v40, v40\n\t"
        "v_exp_f32 v41, v41\n\t"
        "v_smfmac_f32_32x32x32_bf16 v[128:143], v[208:211], v[0:7], v216\n\t"
        "ds_read_b128 v[184:187], v217 offset:4608\n\t"
        "v_exp_f32 v42, v42\n\t"
        "v_exp_f32 v43, v43\n\t"
        "v_exp_f32 v44, v44\n\t"
        "v_smfmac_f32_32x32x32_bf16 v[128:143], v[212:215], v[8:15], v216\n\t"
        "v_exp_f32 v45, v45\n\t"
        "v_exp_f32 v46, v46\n\t"
        "v_exp_f32 v47, v47\n\t"
        "v_mfma_f32_32x32x16_bf16 v[64:79], v[180:183], v[168:171], v[64:79]\n\t"
        "v_exp_f32 v48, v48\n\t"
        "v_exp_f32 v49, v49\n\t"
        "v_exp_f32 v50, v50\n\t"
        "v_mfma_f32_32x32x16_bf16 v[80:95], v[180:183], v[172:175], v[80:95]\n\t"
        "v_exp_f32 v51, v51\n\t"
        "v_exp_f32 v52, v52\n\t"
        "v_exp_f32 v53, v53\n\t"
        "v_smfmac_f32_32x32x32_bf16 v[144:159], v[208:211], v[16:23], v216\n\t"
        "v_exp_f32 v54, v54\n\t"
        "v_exp_f32 v55, v55\n\t"
        "v_exp_f32 v56, v56\n\t"
        "v_smfmac_f32_32x32x32_bf16 v[144:159], v[212:215], v[24:31], v216\n\t"
        "s_waitcnt lgkmcnt(1)\n\t"
        "v_exp_f32 v57, v57\n\t"
        "v_exp_f32 v58, v58\n\t"
        "v_exp_f32 v59, v59\n\t"
        "v_exp_f32 v60, v60\n\t"
        "v_exp_f32 v61, v61\n\t"
        "v_exp_f32 v62, v62\n\t"
        "v_exp_f32 v63, v63\n\t"
        "v_mfma_f32_32x32x4_2b_bf16 v[0:31], v[194:195], v[176:177], 0\n\t"
        "v_exp_f32 v64, v64\n\t"
        "v_exp_f32 v65, v65\n\t"
        "v_exp_f32 v66, v66\n\t"
        "v_exp_f32 v67, v67\n\t"
        "v_exp_f32 v68, v68\n\t"
        "v_exp_f32 v69, v69\n\t"
        "v_exp_f32 v70, v70\n\t"
        "v_exp_f32 v71, v71\n\t"
        "v_exp_f32 v72, v72\n\t"
        "v_exp_f32 v73, v73\n\t"
        "v_smfmac_f32_32x32x32_bf16 v[96:111], v[200:203], v[32:39], v216\n\t"
        "ds_read2st64_b64 v[196:199], v218 offset0:14 offset1:16\n\t"
        "ds_read_b128 v[208:211], v219 offset:5632\n\t"
        "v_exp_f32 v74, v74\n\t"
        "v_exp_f32 v75, v75\n\t"
        "v_exp_f32 v76, v76\n\t"
        "v_smfmac_f32_32x32x32_bf16 v[96:111], v[204:207], v[40:47], v216\n\t"
        "s_waitcnt lgkmcnt(2)\n\t"
        "ds_read_b128 v[212:215], v219 offset:6400\n\t"
        "v_exp_f32 v77, v77\n\t"
        "v_exp_f32 v78, v78\n\t"
        "v_exp_f32 v79, v79\n\t"
        "v_mfma_f32_32x32x16_bf16 v[0:15], v[184:187], v[160:163], v[0:15]\n\t"
        "v_exp_f32 v80, v80\n\t"
        "v_exp_f32 v81, v81\n\t"
        "v_exp_f32 v82, v82\n\t"
        "v_mfma_f32_32x32x16_bf16 v[16:31], v[184:187], v[164:167], v[16:31]\n\t"
        "v_exp_f32 v83, v83\n\t"
        "v_exp_f32 v84, v84\n\t"
        "v_exp_f32 v85, v85\n\t"
        "v_smfmac_f32_32x32x32_bf16 v[112:127], v[200:203], v[48:55], v216\n\t"
        "v_exp_f32 v86, v86\n\t"
        "v_exp_f32 v87, v87\n\t"
        "v_exp_f32 v88, v88\n\t"
        "v_smfmac_f32_32x32x32_bf16 v[112:127], v[204:207], v[56:63], v216\n\t"
        "v_exp_f32 v89, v89\n\t"
        "v_exp_f32 v90, v90\n\t"
        "v_exp_f32 v91, v91\n\t"
        "v_exp_f32 v92, v92\n\t"
        "v_exp_f32 v93, v93\n\t"
        "v_exp_f32 v94, v94\n\t"
        "v_exp_f32 v95, v95\n\t"
        "v_mfma_f32_32x32x4_2b_bf16 v[32:63], v[194:195], v[178:179], 0\n\t"
        "v_exp_f32 v0, v0\n\t"
        "v_exp_f32 v1, v1\n\t"
        "v_exp_f32 v2, v2\n\t"
        "v_exp_f32 v3, v3\n\t"
        "v_exp_f32 v4, v4\n\t"
        "v_exp_f32 v5, v5\n\t"
        "v_exp_f32 v6, v6\n\t"
        "v_exp_f32 v7, v7\n\t"
        "v_exp_f32 v8, v8\n\t"
        "v_exp_f32 v9, v9\n\t"
        "v_smfmac_f32_32x32x32_bf16 v[128:143], v[200:203], v[64:71], v216\n\t"
        "ds_read_b128 v[180:183], v217 offset:9216\n\t"
        "v_exp_f32 v10, v10\n\t"
        "v_exp_f32 v11, v11\n\t"
        "v_exp_f32 v12, v12\n\t"
        "v_smfmac_f32_32x32x32_bf16 v[128:143], v[204:207], v[72:79], v216\n\t"
        "v_exp_f32 v13, v13\n\t"
        "v_exp_f32 v14, v14\n\t"
        "v_exp_f32 v15, v15\n\t"
        "v_mfma_f32_32x32x16_bf16 v[32:47], v[184:187], v[168:171], v[32:47]\n\t"
        "v_exp_f32 v16, v16\n\t"
        "v_exp_f32 v17, v17\n\t"
        "v_exp_f32 v18, v18\n\t"
        "v_mfma_f32_32x32x16_bf16 v[48:63], v[184:187], v[172:175], v[48:63]\n\t"
        "v_exp_f32 v19, v19\n\t"
        "v_exp_f32 v20, v20\n\t"
        "v_exp_f32 v21, v21\n\t"
        "v_smfmac_f32_32x32x32_bf16 v[144:159], v[200:203], v[80:87], v216\n\t"
        "v_exp_f32 v22, v22\n\t"
        "v_exp_f32 v23, v23\n\t"
        "v_exp_f32 v24, v24\n\t"
        "v_smfmac_f32_32x32x32_bf16 v[144:159], v[204:207], v[88:95], v216\n\t"
        "s_waitcnt lgkmcnt(1)\n\t"
        "v_exp_f32 v25, v25\n\t"
        "v_exp_f32 v26, v26\n\t"
        "v_exp_f32 v27, v27\n\t"
        "v_exp_f32 v28, v28\n\t"
        "v_exp_f32 v29, v29\n\t"
        "v_exp_f32 v30, v30\n\t"
        "v_exp_f32 v31, v31\n\t"
        "v_mfma_f32_32x32x4_2b_bf16 v[64:95], v[196:197], v[176:177], 0\n\t"
        "v_exp_f32 v32, v32\n\t"
        "v_exp_f32 v33, v33\n\t"
        "v_exp_f32 v34, v34\n\t"
        "v_exp_f32 v35, v35\n\t"
        "v_exp_f32 v36, v36\n\t"
        "v_exp_f32 v37, v37\n\t"
        "v_exp_f32 v38, v38\n\t"
        "v_exp_f32 v39, v39\n\t"
        "v_exp_f32 v40, v40\n\t"
        "v_exp_f32 v41, v41\n\t"
        "v_smfmac_f32_32x32x32_bf16 v[96:111], v[208:211], v[0:7], v216\n\t"
        "ds_read_b128 v[200:203], v219 offset:7424\n\t"
        "v_exp_f32 v42, v42\n\t"
        "v_exp_f32 v43, v43\n\t"
        "v_exp_f32 v44, v44\n\t"
        "v_smfmac_f32_32x32x32_bf16 v[96:111], v[212:215], v[8:15], v216\n\t"
        "s_waitcnt lgkmcnt(1)\n\t"
        "ds_read_b128 v[204:207], v219 offset:8448\n\t"
        "v_exp_f32 v45, v45\n\t"
        "v_exp_f32 v46, v46\n\t"
        "v_exp_f32 v47, v47\n\t"
        "v_mfma_f32_32x32x16_bf16 v[64:79], v[180:183], v[160:163], v[64:79]\n\t"
        "v_exp_f32 v48, v48\n\t"
        "v_exp_f32 v49, v49\n\t"
        "v_exp_f32 v50, v50\n\t"
        "v_mfma_f32_32x32x16_bf16 v[80:95], v[180:183], v[164:167], v[80:95]\n\t"
        "v_exp_f32 v51, v51\n\t"
        "v_exp_f32 v52, v52\n\t"
        "v_exp_f32 v53, v53\n\t"
        "v_smfmac_f32_32x32x32_bf16 v[112:127], v[208:211], v[16:23], v216\n\t"
        "v_exp_f32 v54, v54\n\t"
        "v_exp_f32 v55, v55\n\t"
        "v_exp_f32 v56, v56\n\t"
        "v_smfmac_f32_32x32x32_bf16 v[112:127], v[212:215], v[24:31], v216\n\t"
        "v_exp_f32 v57, v57\n\t"
        "v_exp_f32 v58, v58\n\t"
        "v_exp_f32 v59, v59\n\t"
        "v_exp_f32 v60, v60\n\t"
        "v_exp_f32 v61, v61\n\t"
        "v_exp_f32 v62, v62\n\t"
        "v_exp_f32 v63, v63\n\t"
        "v_mfma_f32_32x32x4_2b_bf16 v[0:31], v[196:197], v[178:179], 0\n\t"
        "v_exp_f32 v64, v64\n\t"
        "v_exp_f32 v65, v65\n\t"
        "v_exp_f32 v66, v66\n\t"
        "v_exp_f32 v67, v67\n\t"
        "v_exp_f32 v68, v68\n\t"
        "v_exp_f32 v69, v69\n\t"
        "v_exp_f32 v70, v70\n\t"
        "v_exp_f32 v71, v71\n\t"
        "v_exp_f32 v72, v72\n\t"
        "v_exp_f32 v73, v73\n\t"
        "v_smfmac_f32_32x32x32_bf16 v[128:143], v[208:211], v[32:39], v216\n\t"
        "ds_read_b128 v[184:187], v217 offset:10240\n\t"
        "v_exp_f32 v74, v74\n\t"
        "v_exp_f32 v75, v75\n\t"
        "v_exp_f32 v76, v76\n\t"
        "v_smfmac_f32_32x32x32_bf16 v[128:143], v[212:215], v[40:47], v216\n\t"
        "v_exp_f32 v77, v77\n\t"
        "v_exp_f32 v78, v78\n\t"
        "v_exp_f32 v79, v79\n\t"
        "v_mfma_f32_32x32x16_bf16 v[0:15], v[180:183], v[168:171], v[0:15]\n\t"
        "v_exp_f32 v80, v80\n\t"
        "v_exp_f32 v81, v81\n\t"
        "v_exp_f32 v82, v82\n\t"
        "v_mfma_f32_32x32x16_bf16 v[16:31], v[180:183], v[172:175], v[16:31]\n\t"
        "v_exp_f32 v83, v83\n\t"
        "v_exp_f32 v84, v84\n\t"
        "v_exp_f32 v85, v85\n\t"
        "v_smfmac_f32_32x32x32_bf16 v[144:159], v[208:211], v[48:55], v216\n\t"
        "v_exp_f32 v86, v86\n\t"
        "v_exp_f32 v87, v87\n\t"
        "v_exp_f32 v88, v88\n\t"
        "v_smfmac_f32_32x32x32_bf16 v[144:159], v[212:215], v[56:63], v216\n\t"
        "s_waitcnt lgkmcnt(1)\n\t"
        "v_exp_f32 v89, v89\n\t"
        "v_exp_f32 v90, v90\n\t"
        "v_exp_f32 v91, v91\n\t"
        "v_exp_f32 v92, v92\n\t"
        "v_exp_f32 v93, v93\n\t"
        "v_exp_f32 v94, v94\n\t"
        "v_exp_f32 v95, v95\n\t"
        "v_mfma_f32_32x32x4_2b_bf16 v[32:63], v[198:199], v[176:177], 0\n\t"
        "v_exp_f32 v0, v0\n\t"
        "v_exp_f32 v1, v1\n\t"
        "v_exp_f32 v2, v2\n\t"
        "v_exp_f32 v3, v3\n\t"
        "v_exp_f32 v4, v4\n\t"
        "v_exp_f32 v5, v5\n\t"
        "v_exp_f32 v6, v6\n\t"
        "v_exp_f32 v7, v7\n\t"
        "v_exp_f32 v8, v8\n\t"
        "v_exp_f32 v9, v9\n\t"
        "v_smfmac_f32_32x32x32_bf16 v[96:111], v[200:203], v[64:71], v216\n\t"
        "ds_read2st64_b64 v[188:191], v218 offset0:25 offset1:27\n\t"
        "ds_read_b128 v[208:211], v219 offset:11264\n\t"
        "v_exp_f32 v10, v10\n\t"
        "v_exp_f32 v11, v11\n\t"
        "v_exp_f32 v12, v12\n\t"
        "v_smfmac_f32_32x32x32_bf16 v[96:111], v[204:207], v[72:79], v216\n\t"
        "s_waitcnt lgkmcnt(2)\n\t"
        "ds_read_b128 v[212:215], v219 offset:12032\n\t"
        "v_exp_f32 v13, v13\n\t"
        "v_exp_f32 v14, v14\n\t"
        "v_exp_f32 v15, v15\n\t"
        "v_mfma_f32_32x32x16_bf16 v[32:47], v[184:187], v[160:163], v[32:47]\n\t"
        "v_exp_f32 v16, v16\n\t"
        "v_exp_f32 v17, v17\n\t"
        "v_exp_f32 v18, v18\n\t"
        "v_mfma_f32_32x32x16_bf16 v[48:63], v[184:187], v[164:167], v[48:63]\n\t"
        "v_exp_f32 v19, v19\n\t"
        "v_exp_f32 v20, v20\n\t"
        "v_exp_f32 v21, v21\n\t"
        "v_smfmac_f32_32x32x32_bf16 v[112:127], v[200:203], v[80:87], v216\n\t"
        "v_exp_f32 v22, v22\n\t"
        "v_exp_f32 v23, v23\n\t"
        "v_exp_f32 v24, v24\n\t"
        "v_smfmac_f32_32x32x32_bf16 v[112:127], v[204:207], v[88:95], v216\n\t"
        "v_exp_f32 v25, v25\n\t"
        "v_exp_f32 v26, v26\n\t"
        "v_exp_f32 v27, v27\n\t"
        "v_exp_f32 v28, v28\n\t"
        "v_exp_f32 v29, v29\n\t"
        "v_exp_f32 v30, v30\n\t"
        "v_exp_f32 v31, v31\n\t"
        "v_mfma_f32_32x32x4_2b_bf16 v[64:95], v[198:199], v[178:179], 0\n\t"
        "v_exp_f32 v32, v32\n\t"
        "v_exp_f32 v33, v33\n\t"
        "v_exp_f32 v34, v34\n\t"
        "v_exp_f32 v35, v35\n\t"
        "v_exp_f32 v36, v36\n\t"
        "v_exp_f32 v37, v37\n\t"
        "v_exp_f32 v38, v38\n\t"
        "v_exp_f32 v39, v39\n\t"
        "v_exp_f32 v40, v40\n\t"
        "v_exp_f32 v41, v41\n\t"
        "v_smfmac_f32_32x32x32_bf16 v[128:143], v[200:203], v[0:7], v216\n\t"
        "ds_read_b128 v[180:183], v217 offset:14848\n\t"
        "v_exp_f32 v42, v42\n\t"
        "v_exp_f32 v43, v43\n\t"
        "v_exp_f32 v44, v44\n\t"
        "v_smfmac_f32_32x32x32_bf16 v[128:143], v[204:207], v[8:15], v216\n\t"
        "v_exp_f32 v45, v45\n\t"
        "v_exp_f32 v46, v46\n\t"
        "v_exp_f32 v47, v47\n\t"
        "v_mfma_f32_32x32x16_bf16 v[64:79], v[184:187], v[168:171], v[64:79]\n\t"
        "v_exp_f32 v48, v48\n\t"
        "v_exp_f32 v49, v49\n\t"
        "v_exp_f32 v50, v50\n\t"
        "v_mfma_f32_32x32x16_bf16 v[80:95], v[184:187], v[172:175], v[80:95]\n\t"
        "v_exp_f32 v51, v51\n\t"
        "v_exp_f32 v52, v52\n\t"
        "v_exp_f32 v53, v53\n\t"
        "v_smfmac_f32_32x32x32_bf16 v[144:159], v[200:203], v[16:23], v216\n\t"
        "v_exp_f32 v54, v54\n\t"
        "v_exp_f32 v55, v55\n\t"
        "v_exp_f32 v56, v56\n\t"
        "v_smfmac_f32_32x32x32_bf16 v[144:159], v[204:207], v[24:31], v216\n\t"
        "s_waitcnt lgkmcnt(1)\n\t"
        "v_exp_f32 v57, v57\n\t"
        "v_exp_f32 v58, v58\n\t"
        "v_exp_f32 v59, v59\n\t"
        "v_exp_f32 v60, v60\n\t"
        "v_exp_f32 v61, v61\n\t"
        "v_exp_f32 v62, v62\n\t"
        "v_exp_f32 v63, v63\n\t"
        "v_mfma_f32_32x32x4_2b_bf16 v[0:31], v[188:189], v[176:177], 0\n\t"
        "v_exp_f32 v64, v64\n\t"
        "v_exp_f32 v65, v65\n\t"
        "v_exp_f32 v66, v66\n\t"
        "v_exp_f32 v67, v67\n\t"
        "v_exp_f32 v68, v68\n\t"
        "v_exp_f32 v69, v69\n\t"
        "v_exp_f32 v70, v70\n\t"
        "v_exp_f32 v71, v71\n\t"
        "v_exp_f32 v72, v72\n\t"
        "v_exp_f32 v73, v73\n\t"
        "v_smfmac_f32_32x32x32_bf16 v[96:111], v[208:211], v[32:39], v216\n\t"
        "ds_read_b128 v[200:203], v219 offset:13056\n\t"
        "v_exp_f32 v74, v74\n\t"
        "v_exp_f32 v75, v75\n\t"
        "v_exp_f32 v76, v76\n\t"
        "v_smfmac_f32_32x32x32_bf16 v[96:111], v[212:215], v[40:47], v216\n\t"
        "s_waitcnt lgkmcnt(1)\n\t"
        "ds_read_b128 v[204:207], v219 offset:14080\n\t"
        "v_exp_f32 v77, v77\n\t"
        "v_exp_f32 v78, v78\n\t"
        "v_exp_f32 v79, v79\n\t"
        "v_mfma_f32_32x32x16_bf16 v[0:15], v[180:183], v[160:163], v[0:15]\n\t"
        "v_exp_f32 v80, v80\n\t"
        "v_exp_f32 v81, v81\n\t"
        "v_exp_f32 v82, v82\n\t"
        "v_mfma_f32_32x32x16_bf16 v[16:31], v[180:183], v[164:167], v[16:31]\n\t"
        "v_exp_f32 v83, v83\n\t"
        "v_exp_f32 v84, v84\n\t"
        "v_exp_f32 v85, v85\n\t"
        "v_smfmac_f32_32x32x32_bf16 v[112:127], v[208:211], v[48:55], v216\n\t"
        "v_exp_f32 v86, v86\n\t"
        "v_exp_f32 v87, v87\n\t"
        "v_exp_f32 v88, v88\n\t"
        "v_smfmac_f32_32x32x32_bf16 v[112:127], v[212:215], v[56:63], v216\n\t"
        "v_exp_f32 v89, v89\n\t"
        "v_exp_f32 v90, v90\n\t"
        "v_exp_f32 v91, v91\n\t"
        "v_exp_f32 v92, v92\n\t"
        "v_exp_f32 v93, v93\n\t"
        "v_exp_f32 v94, v94\n\t"
        "v_exp_f32 v95, v95\n\t"
        "v_mfma_f32_32x32x4_2b_bf16 v[32:63], v[188:189], v[178:179], 0\n\t"
        "v_exp_f32 v0, v0\n\t"
        "v_exp_f32 v1, v1\n\t"
        "v_exp_f32 v2, v2\n\t"
        "v_exp_f32 v3, v3\n\t"
        "v_exp_f32 v4, v4\n\t"
        "v_exp_f32 v5, v5\n\t"
        "v_exp_f32 v6, v6\n\t"
        "v_exp_f32 v7, v7\n\t"
        "v_exp_f32 v8, v8\n\t"
        "v_exp_f32 v9, v9\n\t"
        "v_smfmac_f32_32x32x32_bf16 v[128:143], v[208:211], v[64:71], v216\n\t"
        "ds_read_b128 v[184:187], v217 offset:15872\n\t"
        "v_exp_f32 v10, v10\n\t"
        "v_exp_f32 v11, v11\n\t"
        "v_exp_f32 v12, v12\n\t"
        "v_smfmac_f32_32x32x32_bf16 v[128:143], v[212:215], v[72:79], v216\n\t"
        "v_exp_f32 v13, v13\n\t"
        "v_exp_f32 v14, v14\n\t"
        "v_exp_f32 v15, v15\n\t"
        "v_mfma_f32_32x32x16_bf16 v[32:47], v[180:183], v[168:171], v[32:47]\n\t"
        "v_exp_f32 v16, v16\n\t"
        "v_exp_f32 v17, v17\n\t"
        "v_exp_f32 v18, v18\n\t"
        "v_mfma_f32_32x32x16_bf16 v[48:63], v[180:183], v[172:175], v[48:63]\n\t"
        "v_exp_f32 v19, v19\n\t"
        "v_exp_f32 v20, v20\n\t"
        "v_exp_f32 v21, v21\n\t"
        "v_smfmac_f32_32x32x32_bf16 v[144:159], v[208:211], v[80:87], v216\n\t"
        "v_exp_f32 v22, v22\n\t"
        "v_exp_f32 v23, v23\n\t"
        "v_exp_f32 v24, v24\n\t"
        "v_smfmac_f32_32x32x32_bf16 v[144:159], v[212:215], v[88:95], v216\n\t"
        "s_waitcnt lgkmcnt(1)\n\t"
        "v_exp_f32 v25, v25\n\t"
        "v_exp_f32 v26, v26\n\t"
        "v_exp_f32 v27, v27\n\t"
        "v_exp_f32 v28, v28\n\t"
        "v_exp_f32 v29, v29\n\t"
        "v_exp_f32 v30, v30\n\t"
        "v_exp_f32 v31, v31\n\t"
        "v_mfma_f32_32x32x4_2b_bf16 v[64:95], v[190:191], v[176:177], 0\n\t"
        "v_exp_f32 v32, v32\n\t"
        "v_exp_f32 v33, v33\n\t"
        "v_exp_f32 v34, v34\n\t"
        "v_exp_f32 v35, v35\n\t"
        "v_exp_f32 v36, v36\n\t"
        "v_exp_f32 v37, v37\n\t"
        "v_exp_f32 v38, v38\n\t"
        "v_exp_f32 v39, v39\n\t"
        "v_exp_f32 v40, v40\n\t"
        "v_exp_f32 v41, v41\n\t"
        "v_smfmac_f32_32x32x32_bf16 v[96:111], v[200:203], v[0:7], v216\n\t"
        "ds_read2st64_b64 v[192:195], v218 offset0:36 offset1:38\n\t"
        "ds_read_b128 v[208:211], v219 offset:16896\n\t"
        "v_exp_f32 v42, v42\n\t"
        "v_exp_f32 v43, v43\n\t"
        "v_exp_f32 v44, v44\n\t"
        "v_smfmac_f32_32x32x32_bf16 v[96:111], v[204:207], v[8:15], v216\n\t"
        "s_waitcnt lgkmcnt(2)\n\t"
        "ds_read_b128 v[212:215], v219 offset:17664\n\t"
        "v_exp_f32 v45, v45\n\t"
        "v_exp_f32 v46, v46\n\t"
        "v_exp_f32 v47, v47\n\t"
        "v_mfma_f32_32x32x16_bf16 v[64:79], v[184:187], v[160:163], v[64:79]\n\t"
        "v_exp_f32 v48, v48\n\t"
        "v_exp_f32 v49, v49\n\t"
        "v_exp_f32 v50, v50\n\t"
        "v_mfma_f32_32x32x16_bf16 v[80:95], v[184:187], v[164:167], v[80:95]\n\t"
        "v_exp_f32 v51, v51\n\t"
        "v_exp_f32 v52, v52\n\t"
        "v_exp_f32 v53, v53\n\t"
        "v_smfmac_f32_32x32x32_bf16 v[112:127], v[200:203], v[16:23], v216\n\t"
        "v_exp_f32 v54, v54\n\t"
        "v_exp_f32 v55, v55\n\t"
        "v_exp_f32 v56, v56\n\t"
        "v_smfmac_f32_32x32x32_bf16 v[112:127], v[204:207], v[24:31], v216\n\t"
        "v_exp_f32 v57, v57\n\t"
        "v_exp_f32 v58, v58\n\t"
        "v_exp_f32 v59, v59\n\t"
        "v_exp_f32 v60, v60\n\t"
        "v_exp_f32 v61, v61\n\t"
        "v_exp_f32 v62, v62\n\t"
        "v_exp_f32 v63, v63\n\t"
        "v_mfma_f32_32x32x4_2b_bf16 v[0:31], v[190:191], v[178:179], 0\n\t"
        "v_exp_f32 v64, v64\n\t"
        "v_exp_f32 v65, v65\n\t"
        "v_exp_f32 v66, v66\n\t"
        "v_exp_f32 v67, v67\n\t"
        "v_exp_f32 v68, v68\n\t"
        "v_exp_f32 v69, v69\n\t"
        "v_exp_f32 v70, v70\n\t"
        "v_exp_f32 v71, v71\n\t"
        "v_exp_f32 v72, v72\n\t"
        "v_exp_f32 v73, v73\n\t"
        "v_smfmac_f32_32x32x32_bf16 v[128:143], v[200:203], v[32:39], v216\n\t"
        "ds_read_b128 v[180:183], v217 offset:20480\n\t"
        "v_exp_f32 v74, v74\n\t"
        "v_exp_f32 v75, v75\n\t"
        "v_exp_f32 v76, v76\n\t"
        "v_smfmac_f32_32x32x32_bf16 v[128:143], v[204:207], v[40:47], v216\n\t"
        "v_exp_f32 v77, v77\n\t"
        "v_exp_f32 v78, v78\n\t"
        "v_exp_f32 v79, v79\n\t"
        "v_mfma_f32_32x32x16_bf16 v[0:15], v[184:187], v[168:171], v[0:15]\n\t"
        "v_exp_f32 v80, v80\n\t"
        "v_exp_f32 v81, v81\n\t"
        "v_exp_f32 v82, v82\n\t"
        "v_mfma_f32_32x32x16_bf16 v[16:31], v[184:187], v[172:175], v[16:31]\n\t"
        "v_exp_f32 v83, v83\n\t"
        "v_exp_f32 v84, v84\n\t"
        "v_exp_f32 v85, v85\n\t"
        "v_smfmac_f32_32x32x32_bf16 v[144:159], v[200:203], v[48:55], v216\n\t"
        "v_exp_f32 v86, v86\n\t"
        "v_exp_f32 v87, v87\n\t"
        "v_exp_f32 v88, v88\n\t"
        "v_smfmac_f32_32x32x32_bf16 v[144:159], v[204:207], v[56:63], v216\n\t"
        "s_waitcnt lgkmcnt(1)\n\t"
        "v_exp_f32 v89, v89\n\t"
        "v_exp_f32 v90, v90\n\t"
        "v_exp_f32 v91, v91\n\t"
        "v_exp_f32 v92, v92\n\t"
        "v_exp_f32 v93, v93\n\t"
        "v_exp_f32 v94, v94\n\t"
        "v_exp_f32 v95, v95\n\t"
        "v_mfma_f32_32x32x4_2b_bf16 v[32:63], v[192:193], v[176:177], 0\n\t"
        "v_exp_f32 v0, v0\n\t"
        "v_exp_f32 v1, v1\n\t"
        "v_exp_f32 v2, v2\n\t"
        "v_exp_f32 v3, v3\n\t"
        "v_exp_f32 v4, v4\n\t"
        "v_exp_f32 v5, v5\n\t"
        "v_exp_f32 v6, v6\n\t"
        "v_exp_f32 v7, v7\n\t"
        "v_exp_f32 v8, v8\n\t"
        "v_exp_f32 v9, v9\n\t"
        "v_smfmac_f32_32x32x32_bf16 v[96:111], v[208:211], v[64:71], v216\n\t"
        "ds_read_b128 v[200:203], v219 offset:18688\n\t"
        "v_exp_f32 v10, v10\n\t"
        "v_exp_f32 v11, v11\n\t"
        "v_exp_f32 v12, v12\n\t"
        "v_smfmac_f32_32x32x32_bf16 v[96:111], v[212:215], v[72:79], v216\n\t"
        "s_waitcnt lgkmcnt(1)\n\t"
        "ds_read_b128 v[204:207], v219 offset:19712\n\t"
        "v_exp_f32 v13, v13\n\t"
        "v_exp_f32 v14, v14\n\t"
        "v_exp_f32 v15, v15\n\t"
        "v_mfma_f32_32x32x16_bf16 v[32:47], v[180:183], v[160:163], v[32:47]\n\t"
        "v_exp_f32 v16, v16\n\t"
        "v_exp_f32 v17, v17\n\t"
        "v_exp_f32 v18, v18\n\t"
        "v_mfma_f32_32x32x16_bf16 v[48:63], v[180:183], v[164:167], v[48:63]\n\t"
        "v_exp_f32 v19, v19\n\t"
        "v_exp_f32 v20, v20\n\t"
        "v_exp_f32 v21, v21\n\t"
        "v_smfmac_f32_32x32x32_bf16 v[112:127], v[208:211], v[80:87], v216\n\t"
        "v_exp_f32 v22, v22\n\t"
        "v_exp_f32 v23, v23\n\t"
        "v_exp_f32 v24, v24\n\t"
        "v_smfmac_f32_32x32x32_bf16 v[112:127], v[212:215], v[88:95], v216\n\t"
        "v_exp_f32 v25, v25\n\t"
        "v_exp_f32 v26, v26\n\t"
        "v_exp_f32 v27, v27\n\t"
        "v_exp_f32 v28, v28\n\t"
        "v_exp_f32 v29, v29\n\t"
        "v_exp_f32 v30, v30\n\t"
        "v_exp_f32 v31, v31\n\t"
        "v_mfma_f32_32x32x4_2b_bf16 v[64:95], v[192:193], v[178:179], 0\n\t"
        "v_exp_f32 v32, v32\n\t"
        "v_exp_f32 v33, v33\n\t"
        "v_exp_f32 v34, v34\n\t"
        "v_exp_f32 v35, v35\n\t"
        "v_exp_f32 v36, v36\n\t"
        "v_exp_f32 v37, v37\n\t"
        "v_exp_f32 v38, v38\n\t"
        "v_exp_f32 v39, v39\n\t"
        "v_exp_f32 v40, v40\n\t"
        "v_exp_f32 v41, v41\n\t"
        "v_smfmac_f32_32x32x32_bf16 v[128:143], v[208:211], v[0:7], v216\n\t"
        "ds_read_b128 v[184:187], v217 offset:21504\n\t"
        "v_exp_f32 v42, v42\n\t"
        "v_exp_f32 v43, v43\n\t"
        "v_exp_f32 v44, v44\n\t"
        "v_smfmac_f32_32x32x32_bf16 v[128:143], v[212:215], v[8:15], v216\n\t"
        "v_exp_f32 v45, v45\n\t"
        "v_exp_f32 v46, v46\n\t"
        "v_exp_f32 v47, v47\n\t"
        "v_mfma_f32_32x32x16_bf16 v[64:79], v[180:183], v[168:171], v[64:79]\n\t"
        "v_exp_f32 v48, v48\n\t"
        "v_exp_f32 v49, v49\n\t"
        "v_exp_f32 v50, v50\n\t"
        "v_mfma_f32_32x32x16_bf16 v[80:95], v[180:183], v[172:175], v[80:95]\n\t"
        "v_exp_f32 v51, v51\n\t"
        "v_exp_f32 v52, v52\n\t"
        "v_exp_f32 v53, v53\n\t"
        "v_smfmac_f32_32x32x32_bf16 v[144:159], v[208:211], v[16:23], v216\n\t"
        "v_exp_f32 v54, v54\n\t"
        "v_exp_f32 v55, v55\n\t"
        "v_exp_f32 v56, v56\n\t"
        "v_smfmac_f32_32x32x32_bf16 v[144:159], v[212:215], v[24:31], v216\n\t"
        "s_waitcnt lgkmcnt(1)\n\t"
        "v_exp_f32 v57, v57\n\t"
        "v_exp_f32 v58, v58\n\t"
        "v_exp_f32 v59, v59\n\t"
        "v_exp_f32 v60, v60\n\t"
        "v_exp_f32 v61, v61\n\t"
        "v_exp_f32 v62, v62\n\t"
        "v_exp_f32 v63, v63\n\t"
        "v_mfma_f32_32x32x4_2b_bf16 v[0:31], v[194:195], v[176:177], 0\n\t"
        "v_exp_f32 v64, v64\n\t"
        "v_exp_f32 v65, v65\n\t"
        "v_exp_f32 v66, v66\n\t"
        "v_exp_f32 v67, v67\n\t"
        "v_exp_f32 v68, v68\n\t"
        "v_exp_f32 v69, v69\n\t"
        "v_exp_f32 v70, v70\n\t"
        "v_exp_f32 v71, v71\n\t"
        "v_exp_f32 v72, v72\n\t"
        "v_exp_f32 v73, v73\n\t"
        "v_smfmac_f32_32x32x32_bf16 v[96:111], v[200:203], v[32:39], v216\n\t"
        "ds_read2st64_b64 v[196:199], v218 offset0:47 offset1:49\n\t"
        "ds_read_b128 v[208:211], v219 offset:22528\n\t"
        "v_exp_f32 v74, v74\n\t"
        "v_exp_f32 v75, v75\n\t"
        "v_exp_f32 v76, v76\n\t"
        "v_smfmac_f32_32x32x32_bf16 v[96:111], v[204:207], v[40:47], v216\n\t"
        "s_waitcnt lgkmcnt(2)\n\t"
        "ds_read_b128 v[212:215], v219 offset:23296\n\t"
        "v_exp_f32 v77, v77\n\t"
        "v_exp_f32 v78, v78\n\t"
        "v_exp_f32 v79, v79\n\t"
        "v_mfma_f32_32x32x16_bf16 v[0:15], v[184:187], v[160:163], v[0:15]\n\t"
        "v_exp_f32 v80, v80\n\t"
        "v_exp_f32 v81, v81\n\t"
        "v_exp_f32 v82, v82\n\t"
        "v_mfma_f32_32x32x16_bf16 v[16:31], v[184:187], v[164:167], v[16:31]\n\t"
        "v_exp_f32 v83, v83\n\t"
        "v_exp_f32 v84, v84\n\t"
        "v_exp_f32 v85, v85\n\t"
        "v_smfmac_f32_32x32x32_bf16 v[112:127], v[200:203], v[48:55], v216\n\t"
        "v_exp_f32 v86, v86\n\t"
        "v_exp_f32 v87, v87\n\t"
        "v_exp_f32 v88, v88\n\t"
        "v_smfmac_f32_32x32x32_bf16 v[112:127], v[204:207], v[56:63], v216\n\t"
        "v_exp_f32 v89, v89\n\t"
        "v_exp_f32 v90, v90\n\t"
        "v_exp_f32 v91, v91\n\t"
        "v_exp_f32 v92, v92\n\t"
        "v_exp_f32 v93, v93\n\t"
        "v_exp_f32 v94, v94\n\t"
        "v_exp_f32 v95, v95\n\t"
        "v_mfma_f32_32x32x4_2b_bf16 v[32:63], v[194:195], v[178:179], 0\n\t"
        "v_exp_f32 v0, v0\n\t"
        "v_exp_f32 v1, v1\n\t"
        "v_exp_f32 v2, v2\n\t"
        "v_exp_f32 v3, v3\n\t"
        "v_exp_f32 v4, v4\n\t"
        "v_exp_f32 v5, v5\n\t"
        "v_exp_f32 v6, v6\n\t"
        "v_exp_f32 v7, v7\n\t"
        "v_exp_f32 v8, v8\n\t"
        "v_exp_f32 v9, v9\n\t"
        "v_smfmac_f32_32x32x32_bf16 v[128:143], v[200:203], v[64:71], v216\n\t"
        "ds_read_b128 v[180:183], v217 offset:26112\n\t"
        "v_exp_f32 v10, v10\n\t"
        "v_exp_f32 v11, v11\n\t"
        "v_exp_f32 v12, v12\n\t"
        "v_smfmac_f32_32x32x32_bf16 v[128:143], v[204:207], v[72:79], v216\n\t"
        "v_exp_f32 v13, v13\n\t"
        "v_exp_f32 v14, v14\n\t"
        "v_exp_f32 v15, v15\n\t"
        "v_mfma_f32_32x32x16_bf16 v[32:47], v[184:187], v[168:171], v[32:47]\n\t"
        "v_exp_f32 v16, v16\n\t"
        "v_exp_f32 v17, v17\n\t"
        "v_exp_f32 v18, v18\n\t"
        "v_mfma_f32_32x32x16_bf16 v[48:63], v[184:187], v[172:175], v[48:63]\n\t"
        "v_exp_f32 v19, v19\n\t"
        "v_exp_f32 v20, v20\n\t"
        "v_exp_f32 v21, v21\n\t"
        "v_smfmac_f32_32x32x32_bf16 v[144:159], v[200:203], v[80:87], v216\n\t"
        "v_exp_f32 v22, v22\n\t"
        "v_exp_f32 v23, v23\n\t"
        "v_exp_f32 v24, v24\n\t"
        "v_smfmac_f32_32x32x32_bf16 v[144:159], v[204:207], v[88:95], v216\n\t"
        "s_waitcnt lgkmcnt(1)\n\t"
        "v_exp_f32 v25, v25\n\t"
        "v_exp_f32 v26, v26\n\t"
        "v_exp_f32 v27, v27\n\t"
        "v_exp_f32 v28, v28\n\t"
        "v_exp_f32 v29, v29\n\t"
        "v_exp_f32 v30, v30\n\t"
        "v_exp_f32 v31, v31\n\t"
        "v_mfma_f32_32x32x4_2b_bf16 v[64:95], v[196:197], v[176:177], 0\n\t"
        "v_exp_f32 v32, v32\n\t"
        "v_exp_f32 v33, v33\n\t"
        "v_exp_f32 v34, v34\n\t"
        "v_exp_f32 v35, v35\n\t"
        "v_exp_f32 v36, v36\n\t"
        "v_exp_f32 v37, v37\n\t"
        "v_exp_f32 v38, v38\n\t"
        "v_exp_f32 v39, v39\n\t"
        "v_exp_f32 v40, v40\n\t"
        "v_exp_f32 v41, v41\n\t"
        "v_smfmac_f32_32x32x32_bf16 v[96:111], v[208:211], v[0:7], v216\n\t"
        "ds_read_b128 v[200:203], v219 offset:24320\n\t"
        "v_exp_f32 v42, v42\n\t"
        "v_exp_f32 v43, v43\n\t"
        "v_exp_f32 v44, v44\n\t"
        "v_smfmac_f32_32x32x32_bf16 v[96:111], v[212:215], v[8:15], v216\n\t"
        "s_waitcnt lgkmcnt(1)\n\t"
        "ds_read_b128 v[204:207], v219 offset:25344\n\t"
        "v_exp_f32 v45, v45\n\t"
        "v_exp_f32 v46, v46\n\t"
        "v_exp_f32 v47, v47\n\t"
        "v_mfma_f32_32x32x16_bf16 v[64:79], v[180:183], v[160:163], v[64:79]\n\t"
        "v_exp_f32 v48, v48\n\t"
        "v_exp_f32 v49, v49\n\t"
        "v_exp_f32 v50, v50\n\t"
        "v_mfma_f32_32x32x16_bf16 v[80:95], v[180:183], v[164:167], v[80:95]\n\t"
        "v_exp_f32 v51, v51\n\t"
        "v_exp_f32 v52, v52\n\t"
        "v_exp_f32 v53, v53\n\t"
        "v_smfmac_f32_32x32x32_bf16 v[112:127], v[208:211], v[16:23], v216\n\t"
        "v_exp_f32 v54, v54\n\t"
        "v_exp_f32 v55, v55\n\t"
        "v_exp_f32 v56, v56\n\t"
        "v_smfmac_f32_32x32x32_bf16 v[112:127], v[212:215], v[24:31], v216\n\t"
        "v_exp_f32 v57, v57\n\t"
        "v_exp_f32 v58, v58\n\t"
        "v_exp_f32 v59, v59\n\t"
        "v_exp_f32 v60, v60\n\t"
        "v_exp_f32 v61, v61\n\t"
        "v_exp_f32 v62, v62\n\t"
        "v_exp_f32 v63, v63\n\t"
        "v_mfma_f32_32x32x4_2b_bf16 v[0:31], v[196:197], v[178:179], 0\n\t"
        "v_exp_f32 v64, v64\n\t"
        "v_exp_f32 v65, v65\n\t"
        "v_exp_f32 v66, v66\n\t"
        "v_exp_f32 v67, v67\n\t"
        "v_exp_f32 v68, v68\n\t"
        "v_exp_f32 v69, v69\n\t"
        "v_exp_f32 v70, v70\n\t"
        "v_exp_f32 v71, v71\n\t"
        "v_exp_f32 v72, v72\n\t"
        "v_exp_f32 v73, v73\n\t"
        "v_smfmac_f32_32x32x32_bf16 v[128:143], v[208:211], v[32:39], v216\n\t"
        "ds_read_b128 v[184:187], v217 offset:27136\n\t"
        "v_exp_f32 v74, v74\n\t"
        "v_exp_f32 v75, v75\n\t"
        "v_exp_f32 v76, v76\n\t"
        "v_smfmac_f32_32x32x32_bf16 v[128:143], v[212:215], v[40:47], v216\n\t"
        "v_exp_f32 v77, v77\n\t"
        "v_exp_f32 v78, v78\n\t"
        "v_exp_f32 v79, v79\n\t"
        "v_mfma_f32_32x32x16_bf16 v[0:15], v[180:183], v[168:171], v[0:15]\n\t"
        "v_exp_f32 v80, v80\n\t"
        "v_exp_f32 v81, v81\n\t"
        "v_exp_f32 v82, v82\n\t"
        "v_mfma_f32_32x32x16_bf16 v[16:31], v[180:183], v[172:175], v[16:31]\n\t"
        "v_exp_f32 v83, v83\n\t"
        "v_exp_f32 v84, v84\n\t"
        "v_exp_f32 v85, v85\n\t"
        "v_smfmac_f32_32x32x32_bf16 v[144:159], v[208:211], v[48:55], v216\n\t"
        "v_exp_f32 v86, v86\n\t"
        "v_exp_f32 v87, v87\n\t"
        "v_exp_f32 v88, v88\n\t"
        "v_smfmac_f32_32x32x32_bf16 v[144:159], v[212:215], v[56:63], v216\n\t"
        "s_waitcnt lgkmcnt(1)\n\t"
        "v_exp_f32 v89, v89\n\t"
        "v_exp_f32 v90, v90\n\t"
        "v_exp_f32 v91, v91\n\t"
        "v_exp_f32 v92, v92\n\t"
        "v_exp_f32 v93, v93\n\t"
        "v_exp_f32 v94, v94\n\t"
        "v_exp_f32 v95, v95\n\t"
        "v_mfma_f32_32x32x4_2b_bf16 v[32:63], v[198:199], v[176:177], 0\n\t"
        "v_exp_f32 v0, v0\n\t"
        "v_exp_f32 v1, v1\n\t"
        "v_exp_f32 v2, v2\n\t"
        "v_exp_f32 v3, v3\n\t"
        "v_exp_f32 v4, v4\n\t"
        "v_exp_f32 v5, v5\n\t"
        "v_exp_f32 v6, v6\n\t"
        "v_exp_f32 v7, v7\n\t"
        "v_exp_f32 v8, v8\n\t"
        "v_exp_f32 v9, v9\n\t"
        "v_smfmac_f32_32x32x32_bf16 v[96:111], v[200:203], v[64:71], v216\n\t"
        "ds_read2st64_b64 v[188:191], v218 offset0:58 offset1:60\n\t"
        "ds_read_b128 v[208:211], v219 offset:28160\n\t"
        "v_exp_f32 v10, v10\n\t"
        "v_exp_f32 v11, v11\n\t"
        "v_exp_f32 v12, v12\n\t"
        "v_smfmac_f32_32x32x32_bf16 v[96:111], v[204:207], v[72:79], v216\n\t"
        "s_waitcnt lgkmcnt(2)\n\t"
        "ds_read_b128 v[212:215], v219 offset:28928\n\t"
        "v_exp_f32 v13, v13\n\t"
        "v_exp_f32 v14, v14\n\t"
        "v_exp_f32 v15, v15\n\t"
        "v_mfma_f32_32x32x16_bf16 v[32:47], v[184:187], v[160:163], v[32:47]\n\t"
        "v_exp_f32 v16, v16\n\t"
        "v_exp_f32 v17, v17\n\t"
        "v_exp_f32 v18, v18\n\t"
        "v_mfma_f32_32x32x16_bf16 v[48:63], v[184:187], v[164:167], v[48:63]\n\t"
        "v_exp_f32 v19, v19\n\t"
        "v_exp_f32 v20, v20\n\t"
        "v_exp_f32 v21, v21\n\t"
        "v_smfmac_f32_32x32x32_bf16 v[112:127], v[200:203], v[80:87], v216\n\t"
        "v_exp_f32 v22, v22\n\t"
        "v_exp_f32 v23, v23\n\t"
        "v_exp_f32 v24, v24\n\t"
        "v_smfmac_f32_32x32x32_bf16 v[112:127], v[204:207], v[88:95], v216\n\t"
        "v_exp_f32 v25, v25\n\t"
        "v_exp_f32 v26, v26\n\t"
        "v_exp_f32 v27, v27\n\t"
        "v_exp_f32 v28, v28\n\t"
        "v_exp_f32 v29, v29\n\t"
        "v_exp_f32 v30, v30\n\t"
        "v_exp_f32 v31, v31\n\t"
        "v_mfma_f32_32x32x4_2b_bf16 v[64:95], v[198:199], v[178:179], 0\n\t"
        "v_exp_f32 v32, v32\n\t"
        "v_exp_f32 v33, v33\n\t"
        "v_exp_f32 v34, v34\n\t"
        "v_exp_f32 v35, v35\n\t"
        "v_exp_f32 v36, v36\n\t"
        "v_exp_f32 v37, v37\n\t"
        "v_exp_f32 v38, v38\n\t"
        "v_exp_f32 v39, v39\n\t"
        "v_exp_f32 v40, v40\n\t"
        "v_exp_f32 v41, v41\n\t"
        "v_smfmac_f32_32x32x32_bf16 v[128:143], v[200:203], v[0:7], v216\n\t"
        "ds_read_b32 v229, v231\n\t"
        "ds_read_b128 v[180:183], v217 offset:31744\n\t"
        "v_exp_f32 v42, v42\n\t"
        "v_exp_f32 v43, v43\n\t"
        "v_exp_f32 v44, v44\n\t"
        "v_smfmac_f32_32x32x32_bf16 v[128:143], v[204:207], v[8:15], v216\n\t"
        "v_exp_f32 v45, v45\n\t"
        "v_exp_f32 v46, v46\n\t"
        "v_exp_f32 v47, v47\n\t"
        "v_mfma_f32_32x32x16_bf16 v[64:79], v[184:187], v[168:171], v[64:79]\n\t"
        "v_exp_f32 v48, v48\n\t"
        "v_exp_f32 v49, v49\n\t"
        "v_exp_f32 v50, v50\n\t"
        "v_mfma_f32_32x32x16_bf16 v[80:95], v[184:187], v[172:175], v[80:95]\n\t"
        "v_exp_f32 v51, v51\n\t"
        "v_exp_f32 v52, v52\n\t"
        "v_exp_f32 v53, v53\n\t"
        "v_smfmac_f32_32x32x32_bf16 v[144:159], v[200:203], v[16:23], v216\n\t"
        "v_exp_f32 v54, v54\n\t"
        "v_exp_f32 v55, v55\n\t"
        "v_exp_f32 v56, v56\n\t"
        "v_smfmac_f32_32x32x32_bf16 v[144:159], v[204:207], v[24:31], v216\n\t"
        "s_waitcnt lgkmcnt(1)\n\t"
        "v_cmp_gt_u32 vcc, %[xp], v229\n\t"
        "v_exp_f32 v57, v57\n\t"
        "v_exp_f32 v58, v58\n\t"
        "v_exp_f32 v59, v59\n\t"
        "v_exp_f32 v60, v60\n\t"
        "v_exp_f32 v61, v61\n\t"
        "v_exp_f32 v62, v62\n\t"
        "v_exp_f32 v63, v63\n\t"
        "v_mfma_f32_32x32x4_2b_bf16 v[0:31], v[188:189], v[176:177], 0\n\t"
        "v_exp_f32 v64, v64\n\t"
        "v_exp_f32 v65, v65\n\t"
        "v_exp_f32 v66, v66\n\t"
        "v_exp_f32 v67, v67\n\t"
        "v_exp_f32 v68, v68\n\t"
        "v_exp_f32 v69, v69\n\t"
        "v_exp_f32 v70, v70\n\t"
        "v_exp_f32 v71, v71\n\t"
        "v_exp_f32 v72, v72\n\t"
        "v_exp_f32 v73, v73\n\t"
        "v_smfmac_f32_32x32x32_bf16 v[96:111], v[208:211], v[32:39], v216\n\t"
        "ds_read_b128 v[200:203], v219 offset:29952\n\t"
        "v_exp_f32 v74, v74\n\t"
        "v_exp_f32 v75, v75\n\t"
        "v_exp_f32 v76, v76\n\t"
        "v_smfmac_f32_32x32x32_bf16 v[96:111], v[212:215], v[40:47], v216\n\t"
        "s_waitcnt lgkmcnt(1)\n\t"
        "ds_read_b128 v[204:207], v219 offset:30976\n\t"
        "v_exp_f32 v77, v77\n\t"
        "v_exp_f32 v78, v78\n\t"
        "v_exp_f32 v79, v79\n\t"
        "v_mfma_f32_32x32x16_bf16 v[0:15], v[180:183], v[160:163], v[0:15]\n\t"
        "v_exp_f32 v80, v80\n\t"
        "v_exp_f32 v81, v81\n\t"
        "v_exp_f32 v82, v82\n\t"
        "v_mfma_f32_32x32x16_bf16 v[16:31], v[180:183], v[164:167], v[16:31]\n\t"
        "v_exp_f32 v83, v83\n\t"
        "v_exp_f32 v84, v84\n\t"
        "v_exp_f32 v85, v85\n\t"
        "v_smfmac_f32_32x32x32_bf16 v[112:127], v[208:211], v[48:55], v216\n\t"
        "v_exp_f32 v86, v86\n\t"
        "v_exp_f32 v87, v87\n\t"
        "v_exp_f32 v88, v88\n\t"
        "v_smfmac_f32_32x32x32_bf16 v[112:127], v[212:215], v[56:63], v216\n\t"
        "v_exp_f32 v89, v89\n\t"
        "v_exp_f32 v90, v90\n\t"
        "v_exp_f32 v91, v91\n\t"
        "v_exp_f32 v92, v92\n\t"
        "v_exp_f32 v93, v93\n\t"
        "v_exp_f32 v94, v94\n\t"
        "v_exp_f32 v95, v95\n\t"
        "v_mfma_f32_32x32x4_2b_bf16 v[32:63], v[188:189], v[178:179], 0\n\t"
        "v_exp_f32 v0, v0\n\t"
        "v_exp_f32 v1, v1\n\t"
        "v_exp_f32 v2, v2\n\t"
        "v_exp_f32 v3, v3\n\t"
        "v_exp_f32 v4, v4\n\t"
        "v_exp_f32 v5, v5\n\t"
        "v_exp_f32 v6, v6\n\t"
        "v_exp_f32 v7, v7\n\t"
        "v_exp_f32 v8, v8\n\t"
        "v_exp_f32 v9, v9\n\t"
        "v_smfmac_f32_32x32x32_bf16 v[128:143], v[208:211], v[64:71], v216\n\t"
        "ds_read_b128 v[184:187], v217 offset:32768\n\t"
        "v_exp_f32 v10, v10\n\t"
        "v_exp_f32 v11, v11\n\t"
        "v_exp_f32 v12, v12\n\t"
        "v_smfmac_f32_32x32x32_bf16 v[128:143], v[212:215], v[72:79], v216\n\t"
        "s_cmp_eq_u64 vcc, 0\n\t"
        "s_cselect_b32 %[t1], %[it], 14\n\t"
        "v_exp_f32 v13, v13\n\t"
        "v_exp_f32 v14, v14\n\t"
        "v_exp_f32 v15, v15\n\t"
        "v_mfma_f32_32x32x16_bf16 v[32:47], v[180:183], v[168:171], v[32:47]\n\t"
        "s_cmp_lt_u32 %[t1], 14\n\t"
        "v_exp_f32 v16, v16\n\t"
        "v_exp_f32 v17, v17\n\t"
        "v_exp_f32 v18, v18\n\t"
        "v_mfma_f32_32x32x16_bf16 v[48:63], v[180:183], v[172:175], v[48:63]\n\t"
        "v_exp_f32 v19, v19\n\t"
        "v_exp_f32 v20, v20\n\t"
        "v_exp_f32 v21, v21\n\t"
        "v_smfmac_f32_32x32x32_bf16 v[144:159], v[208:211], v[80:87], v216\n\t"
        "v_add_u32 v217, %[t0], v217\n\t"
        "v_add_u32 v218, %[t0], v218\n\t"
        "v_add_u32 v219, %[t0], v219\n\t"
        "v_exp_f32 v22, v22\n\t"
        "v_exp_f32 v23, v23\n\t"
        "v_exp_f32 v24, v24\n\t"
        "v_smfmac_f32_32x32x32_bf16 v[144:159], v[212:215], v[88:95], v216\n\t"
        "s_waitcnt lgkmcnt(1)\n\t"
        "v_exp_f32 v25, v25\n\t"
        "v_exp_f32 v26, v26\n\t"
        "v_exp_f32 v27, v27\n\t"
        "v_exp_f32 v28, v28\n\t"
        "v_exp_f32 v29, v29\n\t"
        "v_exp_f32 v30, v30\n\t"
        "v_exp_f32 v31, v31\n\t"
        "s_cbranch_scc1 1b\n\t"
        "s_cmp_lt_u32 %[it], 14\n\t"
        "s_cbranch_scc1 32b\n\t"
        "v_mfma_f32_32x32x4_2b_bf16 v[64:95], v[190:191], v[176:177], 0\n\t"
        "v_exp_f32 v32, v32\n\t"
        "v_exp_f32 v33, v33\n\t"
        "v_exp_f32 v34, v34\n\t"
        "v_exp_f32 v35, v35\n\t"
        "v_exp_f32 v36, v36\n\t"
        "v_exp_f32 v37, v37\n\t"
        "v_exp_f32 v38, v38\n\t"
        "v_exp_f32 v39, v39\n\t"
        "v_smfmac_f32_32x32x32_bf16 v[96:111], v[200:203], v[0:7], v216\n\t"
        "ds_read2st64_b64 v[192:195], v218 offset0:3 offset1:5\n\t"
        "ds_read_b128 v[208:211], v219 offset:0\n\t"
        "v_exp_f32 v40, v40\n\t"
        "v_exp_f32 v41, v41\n\t"
        "v_exp_f32 v42, v42\n\t"
        "v_exp_f32 v43, v43\n\t"
        "v_smfmac_f32_32x32x32_bf16 v[96:111], v[204:207], v[8:15], v216\n\t"
        "s_waitcnt lgkmcnt(2)\n\t"
        "ds_read_b128 v[212:215], v219 offset:768\n\t"
        "v_exp_f32 v44, v44\n\t"
        "v_exp_f32 v45, v45\n\t"
        "v_exp_f32 v46, v46\n\t"
        "v_exp_f32 v47, v47\n\t"
        "v_mfma_f32_32x32x16_bf16 v[64:79], v[184:187], v[160:163], v[64:79]\n\t"
        "v_exp_f32 v48, v48\n\t"
        "v_exp_f32 v49, v49\n\t"
        "v_exp_f32 v50, v50\n\t"
        "v_exp_f32 v51, v51\n\t"
        "v_mfma_f32_32x32x16_bf16 v[80:95], v[184:187], v[164:167], v[80:95]\n\t"
        "v_exp_f32 v52, v52\n\t"
        "v_exp_f32 v53, v53\n\t"
        "v_exp_f32 v54, v54\n\t"
        "v_exp_f32 v55, v55\n\t"
        "v_smfmac_f32_32x32x32_bf16 v[112:127], v[200:203], v[16:23], v216\n\t"
        "v_exp_f32 v56, v56\n\t"
        "v_exp_f32 v57, v57\n\t"
        "v_exp_f32 v58, v58\n\t"
        "v_exp_f32 v59, v59\n\t"
        "v_smfmac_f32_32x32x32_bf16 v[112:127], v[204:207], v[24:31], v216\n\t"
        "v_exp_f32 v60, v60\n\t"
        "v_exp_f32 v61, v61\n\t"
        "v_exp_f32 v62, v62\n\t"
        "v_exp_f32 v63, v63\n\t"
        "v_mfma_f32_32x32x4_2b_bf16 v[0:31], v[190:191], v[178:179], 0\n\t"
        "v_exp_f32 v64, v64\n\t"
        "v_exp_f32 v65, v65\n\t"
        "v_exp_f32 v66, v66\n\t"
        "v_exp_f32 v67, v67\n\t"
        "v_exp_f32 v68, v68\n\t"
        "v_exp_f32 v69, v69\n\t"
        "v_exp_f32 v70, v70\n\t"
        "v_exp_f32 v71, v71\n\t"
        "v_smfmac_f32_32x32x32_bf16 v[128:143], v[200:203], v[32:39], v216\n\t"
        "ds_read_b128 v[180:183], v217 offset:3584\n\t"
        "v_exp_f32 v72, v72\n\t"
        "v_exp_f32 v73, v73\n\t"
        "v_exp_f32 v74, v74\n\t"
        "v_exp_f32 v75, v75\n\t"
        "v_smfmac_f32_32x32x32_bf16 v[128:143], v[204:207], v[40:47], v216\n\t"
        "v_exp_f32 v76, v76\n\t"
        "v_exp_f32 v77, v77\n\t"
        "v_exp_f32 v78, v78\n\t"
        "v_exp_f32 v79, v79\n\t"
        "v_mfma_f32_32x32x16_bf16 v[0:15], v[184:187], v[168:171], v[0:15]\n\t"
        "v_exp_f32 v80, v80\n\t"
        "v_exp_f32 v81, v81\n\t"
        "v_exp_f32 v82, v82\n\t"
        "v_exp_f32 v83, v83\n\t"
        "v_mfma_f32_32x32x16_bf16 v[16:31], v[184:187], v[172:175], v[16:31]\n\t"
        "v_exp_f32 v84, v84\n\t"
        "v_exp_f32 v85, v85\n\t"
        "v_exp_f32 v86, v86\n\t"
        "v_exp_f32 v87, v87\n\t"
        "v_smfmac_f32_32x32x32_bf16 v[144:159], v[200:203], v[48:55], v216\n\t"
        "v_exp_f32 v88, v88\n\t"
        "v_exp_f32 v89, v89\n\t"
        "v_exp_f32 v90, v90\n\t"
        "v_exp_f32 v91, v91\n\t"
        "v_smfmac_f32_32x32x32_bf16 v[144:159], v[204:207], v[56:63], v216\n\t"
        "s_waitcnt lgkmcnt(1)\n\t"
        "v_exp_f32 v92, v92\n\t"
        "v_exp_f32 v93, v93\n\t"
        "v_exp_f32 v94, v94\n\t"
        "v_exp_f32 v95, v95\n\t"
        "v_mfma_f32_32x32x4_2b_bf16 v[32:63], v[192:193], v[176:177], 0\n\t"
        "v_exp_f32 v0, v0\n\t"
        "v_exp_f32 v1, v1\n\t"
        "v_exp_f32 v2, v2\n\t"
        "v_exp_f32 v3, v3\n\t"
        "v_exp_f32 v4, v4\n\t"
        "v_exp_f32 v5, v5\n\t"
        "v_exp_f32 v6, v6\n\t"
        "v_exp_f32 v7, v7\n\t"
        "v_smfmac_f32_32x32x32_bf16 v[96:111], v[208:211], v[64:71], v216\n\t"
        "ds_read_b128 v[200:203], v219 offset:1792\n\t"
        "v_exp_f32 v8, v8\n\t"
        "v_exp_f32 v9, v9\n\t"
        "v_exp_f32 v10, v10\n\t"
        "v_exp_f32 v11, v11\n\t"
        "v_smfmac_f32_32x32x32_bf16 v[96:111], v[212:215], v[72:79], v216\n\t"
        "s_waitcnt lgkmcnt(1)\n\t"
        "ds_read_b128 v[204:207], v219 offset:2816\n\t"
        "v_exp_f32 v12, v12\n\t"
        "v_exp_f32 v13, v13\n\t"
        "v_exp_f32 v14, v14\n\t"
        "v_exp_f32 v15, v15\n\t"
        "v_mfma_f32_32x32x16_bf16 v[32:47], v[180:183], v[160:163], v[32:47]\n\t"
        "v_exp_f32 v16, v16\n\t"
        "v_exp_f32 v17, v17\n\t"
        "v_exp_f32 v18, v18\n\t"
        "v_exp_f32 v19, v19\n\t"
        "v_mfma_f32_32x32x16_bf16 v[48:63], v[180:183], v[164:167], v[48:63]\n\t"
        "v_exp_f32 v20, v20\n\t"
        "v_exp_f32 v21, v21\n\t"
        "v_exp_f32 v22, v22\n\t"
        "v_exp_f32 v23, v23\n\t"
        "v_smfmac_f32_32x32x32_bf16 v[112:127], v[208:211], v[80:87], v216\n\t"
        "v_exp_f32 v24, v24\n\t"
        "v_exp_f32 v25, v25\n\t"
        "v_exp_f32 v26, v26\n\t"
        "v_exp_f32 v27, v27\n\t"
        "v_smfmac_f32_32x32x32_bf16 v[112:127], v[212:215], v[88:95], v216\n\t"
        "v_exp_f32 v28, v28\n\t"
        "v_exp_f32 v29, v29\n\t"
        "v_exp_f32 v30, v30\n\t"
        "v_exp_f32 v31, v31\n\t"
        "v_mfma_f32_32x32x4_2b_bf16 v[64:95], v[192:193], v[178:179], 0\n\t"
        "v_exp_f32 v32, v32\n\t"
        "v_exp_f32 v33, v33\n\t"
        "v_exp_f32 v34, v34\n\t"
        "v_exp_f32 v35, v35\n\t"
        "v_exp_f32 v36, v36\n\t"
        "v_exp_f32 v37, v37\n\t"
        "v_exp_f32 v38, v38\n\t"
        "v_exp_f32 v39, v39\n\t"
        "v_smfmac_f32_32x32x32_bf16 v[128:143], v[208:211], v[0:7], v216\n\t"
        "ds_read_b128 v[184:187], v217 offset:4608\n\t"
        "v_exp_f32 v40, v40\n\t"
        "v_exp_f32 v41, v41\n\t"
        "v_exp_f32 v42, v42\n\t"
        "v_exp_f32 v43, v43\n\t"
        "v_smfmac_f32_32x32x32_bf16 v[128:143], v[212:215], v[8:15], v216\n\t"
        "v_exp_f32 v44, v44\n\t"
        "v_exp_f32 v45, v45\n\t"
        "v_exp_f32 v46, v46\n\t"
        "v_exp_f32 v47, v47\n\t"
        "v_mfma_f32_32x32x16_bf16 v[64:79], v[180:183], v[168:171], v[64:79]\n\t"
        "v_exp_f32 v48, v48\n\t"
        "v_exp_f32 v49, v49\n\t"
        "v_exp_f32 v50, v50\n\t"
        "v_exp_f32 v51, v51\n\t"
        "v_mfma_f32_32x32x16_bf16 v[80:95], v[180:183], v[172:175], v[80:95]\n\t"
        "v_exp_f32 v52, v52\n\t"
        "v_exp_f32 v53, v53\n\t"
        "v_exp_f32 v54, v54\n\t"
        "v_exp_f32 v55, v55\n\t"
        "v_smfmac_f32_32x32x32_bf16 v[144:159], v[208:211], v[16:23], v216\n\t"
        "v_exp_f32 v56, v56\n\t"
        "v_exp_f32 v57, v57\n\t"
        "v_exp_f32 v58, v58\n\t"
        "v_exp_f32 v59, v59\n\t"
        "v_smfmac_f32_32x32x32_bf16 v[144:159], v[212:215], v[24:31], v216\n\t"
        "s_waitcnt lgkmcnt(1)\n\t"
        "v_exp_f32 v60, v60\n\t"
        "v_exp_f32 v61, v61\n\t"
        "v_exp_f32 v62, v62\n\t"
        "v_exp_f32 v63, v63\n\t"
        "v_mfma_f32_32x32x4_2b_bf16 v[0:31], v[194:195], v[176:177], 0\n\t"
        "v_exp_f32 v64, v64\n\t"
        "v_exp_f32 v65, v65\n\t"
        "v_exp_f32 v66, v66\n\t"
        "v_exp_f32 v67, v67\n\t"
        "v_exp_f32 v68, v68\n\t"
        "v_exp_f32 v69, v69\n\t"
        "v_exp_f32 v70, v70\n\t"
        "v_exp_f32 v71, v71\n\t"
        "v_smfmac_f32_32x32x32_bf16 v[96:111], v[200:203], v[32:39], v216\n\t"
        "ds_read2st64_b64 v[196:199], v218 offset0:33 offset1:35\n\t"
        "ds_read_b128 v[208:211], v219 offset:5632\n\t"
        "v_exp_f32 v72, v72\n\t"
        "v_exp_f32 v73, v73\n\t"
        "v_exp_f32 v74, v74\n\t"
        "v_exp_f32 v75, v75\n\t"
        "v_smfmac_f32_32x32x32_bf16 v[96:111], v[204:207], v[40:47], v216\n\t"
        "s_waitcnt lgkmcnt(2)\n\t"
        "ds_read_b128 v[212:215], v219 offset:6400\n\t"
        "v_exp_f32 v76, v76\n\t"
        "v_exp_f32 v77, v77\n\t"
        "v_exp_f32 v78, v78\n\t"
        "v_exp_f32 v79, v79\n\t"
        "v_mfma_f32_32x32x16_bf16 v[0:15], v[184:187], v[160:163], v[0:15]\n\t"
        "v_exp_f32 v80, v80\n\t"
        "v_exp_f32 v81, v81\n\t"
        "v_exp_f32 v82, v82\n\t"
        "v_exp_f32 v83, v83\n\t"
        "v_mfma_f32_32x32x16_bf16 v[16:31], v[184:187], v[164:167], v[16:31]\n\t"
        "v_exp_f32 v84, v84\n\t"
        "v_exp_f32 v85, v85\n\t"
        "v_exp_f32 v86, v86\n\t"
        "v_exp_f32 v87, v87\n\t"
        "v_smfmac_f32_32x32x32_bf16 v[112:127], v[200:203], v[48:55], v216\n\t"
        "v_exp_f32 v88, v88\n\t"
        "v_exp_f32 v89, v89\n\t"
        "v_exp_f32 v90, v90\n\t"
        "v_exp_f32 v91, v91\n\t"
        "v_smfmac_f32_32x32x32_bf16 v[112:127], v[204:207], v[56:63], v216\n\t"
        "v_exp_f32 v92, v92\n\t"
        "v_exp_f32 v93, v93\n\t"
        "v_exp_f32 v94, v94\n\t"
        "v_exp_f32 v95, v95\n\t"
        "v_mfma_f32_32x32x4_2b_bf16 v[32:63], v[194:195], v[178:179], 0\n\t"
        "v_exp_f32 v0, v0\n\t"
        "v_exp_f32 v1, v1\n\t"
        "v_exp_f32 v2, v2\n\t"
        "v_exp_f32 v3, v3\n\t"
        "v_exp_f32 v4, v4\n\t"
        "v_exp_f32 v5, v5\n\t"
        "v_exp_f32 v6, v6\n\t"
        "v_exp_f32 v7, v7\n\t"
        "v_smfmac_f32_32x32x32_bf16 v[128:143], v[200:203], v[64:71], v216\n\t"
        "ds_read_b128 v[180:183], v217 offset:18944\n\t"
        "v_exp_f32 v8, v8\n\t"
        "v_exp_f32 v9, v9\n\t"
        "v_exp_f32 v10, v10\n\t"
        "v_exp_f32 v11, v11\n\t"
        "v_smfmac_f32_32x32x32_bf16 v[128:143], v[204:207], v[72:79], v216\n\t"
        "v_exp_f32 v12, v12\n\t"
        "v_exp_f32 v13, v13\n\t"
        "v_exp_f32 v14, v14\n\t"
        "v_exp_f32 v15, v15\n\t"
        "v_mfma_f32_32x32x16_bf16 v[32:47], v[184:187], v[168:171], v[32:47]\n\t"
        "v_exp_f32 v16, v16\n\t"
        "v_exp_f32 v17, v17\n\t"
        "v_exp_f32 v18, v18\n\t"
        "v_exp_f32 v19, v19\n\t"
        "v_mfma_f32_32x32x16_bf16 v[48:63], v[184:187], v[172:175], v[48:63]\n\t"
        "v_exp_f32 v20, v20\n\t"
        "v_exp_f32 v21, v21\n\t"
        "v_exp_f32 v22, v22\n\t"
        "v_exp_f32 v23, v23\n\t"
        "v_smfmac_f32_32x32x32_bf16 v[144:159], v[200:203], v[80:87], v216\n\t"
        "v_exp_f32 v24, v24\n\t"
        "v_exp_f32 v25, v25\n\t"
        "v_exp_f32 v26, v26\n\t"
        "v_exp_f32 v27, v27\n\t"
        "v_smfmac_f32_32x32x32_bf16 v[144:159], v[204:207], v[88:95], v216\n\t"
        "s_waitcnt lgkmcnt(1)\n\t"
        "v_exp_f32 v28, v28\n\t"
        "v_exp_f32 v29, v29\n\t"
        "v_exp_f32 v30, v30\n\t"
        "v_exp_f32 v31, v31\n\t"
        "v_mfma_f32_32x32x4_2b_bf16 v[64:95], v[196:197], v[178:179], 0\n\t"
        "v_exp_f32 v32, v32\n\t"
        "v_exp_f32 v33, v33\n\t"
        "v_exp_f32 v34, v34\n\t"
        "v_exp_f32 v35, v35\n\t"
        "v_exp_f32 v36, v36\n\t"
        "v_exp_f32 v37, v37\n\t"
        "v_exp_f32 v38, v38\n\t"
        "v_exp_f32 v39, v39\n\t"
        "v_smfmac_f32_32x32x32_bf16 v[96:111], v[208:211], v[0:7], v216\n\t"
        "ds_read_b128 v[184:187], v217 offset:19968\n\t"
        "v_exp_f32 v40, v40\n\t"
        "v_exp_f32 v41, v41\n\t"
        "v_exp_f32 v42, v42\n\t"
        "v_exp_f32 v43, v43\n\t"
        "v_smfmac_f32_32x32x32_bf16 v[96:111], v[212:215], v[8:15], v216\n\t"
        "s_waitcnt lgkmcnt(1)\n\t"
        "v_exp_f32 v44, v44\n\t"
        "v_exp_f32 v45, v45\n\t"
        "v_exp_f32 v46, v46\n\t"
        "v_exp_f32 v47, v47\n\t"
        "v_mfma_f32_32x32x16_bf16 v[64:79], v[180:183], v[168:171], v[64:79]\n\t"
        "v_exp_f32 v48, v48\n\t"
        "v_exp_f32 v49, v49\n\t"
        "v_exp_f32 v50, v50\n\t"
        "v_exp_f32 v51, v51\n\t"
        "v_mfma_f32_32x32x16_bf16 v[80:95], v[180:183], v[172:175], v[80:95]\n\t"
        "v_exp_f32 v52, v52\n\t"
        "v_exp_f32 v53, v53\n\t"
        "v_exp_f32 v54, v54\n\t"
        "v_exp_f32 v55, v55\n\t"
        "v_smfmac_f32_32x32x32_bf16 v[112:127], v[208:211], v[16:23], v216\n\t"
        "s_waitcnt vmcnt(0)\n\t"
        "v_exp_f32 v56, v56\n\t"
        "v_exp_f32 v57, v57\n\t"
        "v_exp_f32 v58, v58\n\t"
        "v_exp_f32 v59, v59\n\t"
        "v_smfmac_f32_32x32x32_bf16 v[112:127], v[212:215], v[24:31], v216\n\t"
        "v_exp_f32 v60, v60\n\t"
        "v_exp_f32 v61, v61\n\t"
        "v_exp_f32 v62, v62\n\t"
        "v_exp_f32 v63, v63\n\t"
        "v_mfma_f32_32x32x4_2b_bf16 v[0:31], v[198:199], v[178:179], 0\n\t"
        "v_subrev_u32 v229, %[lo], v106\n\t"
        "v_rcp_f32 v106, v106\n\t"
        "s_nop 0\n\t"
        "v_mul_f32 v106, v220, v106\n\t"
        "v_pk_mul_f32 v[96:97], v[96:97], v[106:107] op_sel_hi:[1,0]\n\t"
        "v_pk_mul_f32 v[98:99], v[98:99], v[106:107] op_sel_hi:[1,0]\n\t"
        "v_pk_mul_f32 v[100:101], v[100:101], v[106:107] op_sel_hi:[1,0]\n\t"
        "v_pk_mul_f32 v[102:103], v[102:103], v[106:107] op_sel_hi:[1,0]\n\t"
        "v_pk_mul_f32 v[104:105], v[104:105], v[106:107] op_sel_hi:[1,0]\n\t"
        "v_cvt_pk_bf16_f32 v108, v96, v97\n\t"
        "v_cvt_pk_bf16_f32 v109, v98, v99\n\t"
        "v_cvt_pk_bf16_f32 v110, v100, v101\n\t"
        "v_cvt_pk_bf16_f32 v111, v102, v103\n\t"
        "v_cvt_pk_bf16_f32 v224, v104, v105\n\t"
        "v_subrev_u32 v230, %[lo], v122\n\t"
        "v_rcp_f32 v122, v122\n\t"
        "s_nop 0\n\t"
        "v_mul_f32 v122, v220, v122\n\t"
        "v_pk_mul_f32 v[112:113], v[112:113], v[122:123] op_sel_hi:[1,0]\n\t"
        "v_pk_mul_f32 v[114:115], v[114:115], v[122:123] op_sel_hi:[1,0]\n\t"
        "v_pk_mul_f32 v[116:117], v[116:117], v[122:123] op_sel_hi:[1,0]\n\t"
        "v_pk_mul_f32 v[118:119], v[118:119], v[122:123] op_sel_hi:[1,0]\n\t"
        "v_pk_mul_f32 v[120:121], v[120:121], v[122:123] op_sel_hi:[1,0]\n\t"
        "v_cvt_pk_bf16_f32 v124, v112, v113\n\t"
        "v_cvt_pk_bf16_f32 v125, v114, v115\n\t"
        "v_cvt_pk_bf16_f32 v126, v116, v117\n\t"
        "v_cvt_pk_bf16_f32 v127, v118, v119\n\t"
        "v_cvt_pk_bf16_f32 v225, v120, v121\n\t"
        "v_smfmac_f32_32x32x32_bf16 v[128:143], v[208:211], v[32:39], v216\n\t"
        "buffer_store_dwordx4 v[108:111], v221, %[srdws], 0 offen offset:0 sc1\n\t"
        "ds_read2st64_b64 v[188:191], v218 offset0:44 offset1:46\n\t"
        "ds_read_b128 v[200:203], v219 offset:17152\n\t"
        "v_exp_f32 v64, v64\n\t"
        "v_exp_f32 v65, v65\n\t"
        "v_exp_f32 v66, v66\n\t"
        "v_exp_f32 v67, v67\n\t"
        "v_smfmac_f32_32x32x32_bf16 v[128:143], v[212:215], v[40:47], v216\n\t"
        "buffer_store_dwordx4 v[124:127], v221, %[srdws], 0 offen offset:1024 sc1\n\t"
        "s_waitcnt lgkmcnt(2)\n\t"
        "ds_read_b128 v[204:207], v219 offset:18176\n\t"
        "v_exp_f32 v68, v68\n\t"
        "v_exp_f32 v69, v69\n\t"
        "v_exp_f32 v70, v70\n\t"
        "v_exp_f32 v71, v71\n\t"
        "v_exp_f32 v72, v72\n\t"
        "v_exp_f32 v73, v73\n\t"
        "v_mfma_f32_32x32x16_bf16 v[0:15], v[184:187], v[168:171], v[0:15]\n\t"
        "buffer_store_dwordx2 v[224:225], v233, %[srdws], 0 offen offset:0 sc1\n\t"
        "ds_read_b128 v[180:183], v217 offset:24576\n\t"
        "v_exp_f32 v74, v74\n\t"
        "v_exp_f32 v75, v75\n\t"
        "v_exp_f32 v76, v76\n\t"
        "v_exp_f32 v77, v77\n\t"
        "v_exp_f32 v78, v78\n\t"
        "v_mfma_f32_32x32x16_bf16 v[16:31], v[184:187], v[172:175], v[16:31]\n\t"
        "v_exp_f32 v79, v79\n\t"
        "v_exp_f32 v80, v80\n\t"
        "v_exp_f32 v81, v81\n\t"
        "v_exp_f32 v82, v82\n\t"
        "v_exp_f32 v83, v83\n\t"
        "v_smfmac_f32_32x32x32_bf16 v[144:159], v[208:211], v[48:55], v216\n\t"
        "v_max_u32 v229, v229, v230\n\t"
        "v_cmp_lt_u32 %[badx], %[span], v229\n\t"
        "v_exp_f32 v84, v84\n\t"
        "v_exp_f32 v85, v85\n\t"
        "v_exp_f32 v86, v86\n\t"
        "v_exp_f32 v87, v87\n\t"
        "v_exp_f32 v88, v88\n\t"
        "v_exp_f32 v89, v89\n\t"
        "v_smfmac_f32_32x32x32_bf16 v[144:159], v[212:215], v[56:63], v216\n\t"
        "s_waitcnt lgkmcnt(1)\n\t"
        "v_exp_f32 v90, v90\n\t"
        "v_exp_f32 v91, v91\n\t"
        "v_exp_f32 v92, v92\n\t"
        "v_exp_f32 v93, v93\n\t"
        "v_exp_f32 v94, v94\n\t"
        "v_exp_f32 v95, v95\n\t"
        "v_mfma_f32_32x32x4_2b_bf16 v[32:63], v[188:189], v[178:179], 0\n\t"
        "v_exp_f32 v0, v0\n\t"
        "v_exp_f32 v1, v1\n\t"
        "v_exp_f32 v2, v2\n\t"
        "v_exp_f32 v3, v3\n\t"
        "v_exp_f32 v4, v4\n\t"
        "v_exp_f32 v5, v5\n\t"
        "v_exp_f32 v6, v6\n\t"
        "v_exp_f32 v7, v7\n\t"
        "v_smfmac_f32_32x32x32_bf16 v[128:143], v[200:203], v[64:71], v216\n\t"
        "ds_read_b128 v[208:211], v219 offset:20992\n\t"
        "ds_read_b128 v[212:215], v219 offset:21760\n\t"
        "v_exp_f32 v8, v8\n\t"
        "v_exp_f32 v9, v9\n\t"
        "v_exp_f32 v10, v10\n\t"
        "v_exp_f32 v11, v11\n\t"
        "v_smfmac_f32_32x32x32_bf16 v[128:143], v[204:207], v[72:79], v216\n\t"
        "s_waitcnt lgkmcnt(2)\n\t"
        "ds_read_b128 v[184:187], v217 offset:25600\n\t"
        "v_exp_f32 v12, v12\n\t"
        "v_exp_f32 v13, v13\n\t"
        "v_exp_f32 v14, v14\n\t"
        "v_exp_f32 v15, v15\n\t"
        "v_mfma_f32_32x32x16_bf16 v[32:47], v[180:183], v[168:171], v[32:47]\n\t"
        "s_cmp_lg_u64 %[badx], 0\n\t"
        "v_exp_f32 v16, v16\n\t"
        "v_exp_f32 v17, v17\n\t"
        "v_exp_f32 v18, v18\n\t"
        "v_exp_f32 v19, v19\n\t"
        "v_mfma_f32_32x32x16_bf16 v[48:63], v[180:183], v[172:175], v[48:63]\n\t"
        "s_cselect_b32 %[t0], -1, 0\n\t"
        "v_exp_f32 v20, v20\n\t"
        "v_exp_f32 v21, v21\n\t"
        "v_exp_f32 v22, v22\n\t"
        "v_exp_f32 v23, v23\n\t"
        "v_smfmac_f32_32x32x32_bf16 v[144:159], v[200:203], v[80:87], v216\n\t"
        "s_xor_b32 %[t0], %[t0], s68\n\t"
        "v_mov_b32 v230, %[t0]\n\t"
        "v_mov_b32 v231, s69\n\t"
        "v_exp_f32 v24, v24\n\t"
        "v_exp_f32 v25, v25\n\t"
        "v_exp_f32 v26, v26\n\t"
        "v_exp_f32 v27, v27\n\t"
        "v_smfmac_f32_32x32x32_bf16 v[144:159], v[204:207], v[88:95], v216\n\t"
        "s_waitcnt lgkmcnt(1)\n\t"
        "v_exp_f32 v28, v28\n\t"
        "v_exp_f32 v29, v29\n\t"
        "v_exp_f32 v30, v30\n\t"
        "v_exp_f32 v31, v31\n\t"
        "v_mfma_f32_32x32x4_2b_bf16 v[64:95], v[190:191], v[178:179], 0\n\t"
        "v_exp_f32 v32, v32\n\t"
        "v_exp_f32 v33, v33\n\t"
        "v_exp_f32 v34, v34\n\t"
        "v_exp_f32 v35, v35\n\t"
        "v_exp_f32 v36, v36\n\t"
        "v_exp_f32 v37, v37\n\t"
        "v_exp_f32 v38, v38\n\t"
        "v_exp_f32 v39, v39\n\t"
        "v_smfmac_f32_32x32x32_bf16 v[128:143], v[208:211], v[0:7], v216\n\t"
        "ds_read2st64_b64 v[192:195], v218 offset0:55 offset1:57\n\t"
        "ds_read_b128 v[200:203], v219 offset:22784\n\t"
        "v_exp_f32 v40, v40\n\t"
        "v_exp_f32 v41, v41\n\t"
        "v_exp_f32 v42, v42\n\t"
        "v_exp_f32 v43, v43\n\t"
        "v_smfmac_f32_32x32x32_bf16 v[128:143], v[212:215], v[8:15], v216\n\t"
        "s_waitcnt lgkmcnt(2)\n\t"
        "ds_read_b128 v[204:207], v219 offset:23808\n\t"
        "v_exp_f32 v44, v44\n\t"
        "v_exp_f32 v45, v45\n\t"
        "v_exp_f32 v46, v46\n\t"
        "v_exp_f32 v47, v47\n\t"
        "v_mfma_f32_32x32x16_bf16 v[64:79], v[184:187], v[168:171], v[64:79]\n\t"
        "ds_read_b128 v[180:183], v217 offset:30208\n\t"
        "v_exp_f32 v48, v48\n\t"
        "v_exp_f32 v49, v49\n\t"
        "v_exp_f32 v50, v50\n\t"
        "v_exp_f32 v51, v51\n\t"
        "v_mfma_f32_32x32x16_bf16 v[80:95], v[184:187], v[172:175], v[80:95]\n\t"
        "v_exp_f32 v52, v52\n\t"
        "v_exp_f32 v53, v53\n\t"
        "v_exp_f32 v54, v54\n\t"
        "v_exp_f32 v55, v55\n\t"
        "v_smfmac_f32_32x32x32_bf16 v[144:159], v[208:211], v[16:23], v216\n\t"
        "s_waitcnt vmcnt(0)\n\t"
        "buffer_store_dwordx2 v[230:231], v228, %[srdws], 0 offen sc1\n\t"
        "ds_write_b32 v222, v223\n\t"
        "v_exp_f32 v56, v56\n\t"
        "v_exp_f32 v57, v57\n\t"
        "v_exp_f32 v58, v58\n\t"
        "v_exp_f32 v59, v59\n\t"
        "v_smfmac_f32_32x32x32_bf16 v[144:159], v[212:215], v[24:31], v216\n\t"
        "s_waitcnt lgkmcnt(1)\n\t"
        "v_exp_f32 v60, v60\n\t"
        "v_exp_f32 v61, v61\n\t"
        "v_exp_f32 v62, v62\n\t"
        "v_exp_f32 v63, v63\n\t"
        "v_mfma_f32_32x32x4_2b_bf16 v[0:31], v[192:193], v[178:179], 0\n\t"
        "v_exp_f32 v64, v64\n\t"
        "v_exp_f32 v65, v65\n\t"
        "v_exp_f32 v66, v66\n\t"
        "v_exp_f32 v67, v67\n\t"
        "v_exp_f32 v68, v68\n\t"
        "v_exp_f32 v69, v69\n\t"
        "v_exp_f32 v70, v70\n\t"
        "v_exp_f32 v71, v71\n\t"
        "v_smfmac_f32_32x32x32_bf16 v[128:143], v[200:203], v[32:39], v216\n\t"
        "ds_read_b128 v[208:211], v219 offset:26624\n\t"
        "ds_read_b128 v[212:215], v219 offset:27392\n\t"
        "v_exp_f32 v72, v72\n\t"
        "v_exp_f32 v73, v73\n\t"
        "v_exp_f32 v74, v74\n\t"
        "v_exp_f32 v75, v75\n\t"
        "v_smfmac_f32_32x32x32_bf16 v[128:143], v[204:207], v[40:47], v216\n\t"
        "s_waitcnt lgkmcnt(2)\n\t"
        "ds_read_b128 v[184:187], v217 offset:31232\n\t"
        "v_exp_f32 v76, v76\n\t"
        "v_exp_f32 v77, v77\n\t"
        "v_exp_f32 v78, v78\n\t"
        "v_exp_f32 v79, v79\n\t"
        "v_mfma_f32_32x32x16_bf16 v[0:15], v[180:183], v[168:171], v[0:15]\n\t"
        "v_exp_f32 v80, v80\n\t"
        "v_exp_f32 v81, v81\n\t"
        "v_exp_f32 v82, v82\n\t"
        "v_exp_f32 v83, v83\n\t"
        "v_mfma_f32_32x32x16_bf16 v[16:31], v[180:183], v[172:175], v[16:31]\n\t"
        "v_exp_f32 v84, v84\n\t"
        "v_exp_f32 v85, v85\n\t"
        "v_exp_f32 v86, v86\n\t"
        "v_exp_f32 v87, v87\n\t"
        "v_smfmac_f32_32x32x32_bf16 v[144:159], v[200:203], v[48:55], v216\n\t"
        "v_exp_f32 v88, v88\n\t"
        "v_exp_f32 v89, v89\n\t"
        "v_exp_f32 v90, v90\n\t"
        "v_exp_f32 v91, v91\n\t"
        "v_smfmac_f32_32x32x32_bf16 v[144:159], v[204:207], v[56:63], v216\n\t"
        "s_waitcnt lgkmcnt(1)\n\t"
        "v_exp_f32 v92, v92\n\t"
        "v_exp_f32 v93, v93\n\t"
        "v_exp_f32 v94, v94\n\t"
        "v_exp_f32 v95, v95\n\t"
        "v_mfma_f32_32x32x4_2b_bf16 v[32:63], v[194:195], v[178:179], 0\n\t"
        "v_exp_f32 v0, v0\n\t"
        "v_exp_f32 v1, v1\n\t"
        "v_exp_f32 v2, v2\n\t"
        "v_exp_f32 v3, v3\n\t"
        "v_exp_f32 v4, v4\n\t"
        "v_exp_f32 v5, v5\n\t"
        "v_exp_f32 v6, v6\n\t"
        "v_exp_f32 v7, v7\n\t"
        "v_smfmac_f32_32x32x32_bf16 v[128:143], v[208:211], v[64:71], v216\n\t"
        "ds_read2st64_b64 v[196:199], v218 offset0:66 offset1:68\n\t"
        "ds_read_b128 v[200:203], v219 offset:28416\n\t"
        "v_exp_f32 v8, v8\n\t"
        "v_exp_f32 v9, v9\n\t"
        "v_exp_f32 v10, v10\n\t"
        "v_exp_f32 v11, v11\n\t"
        "v_smfmac_f32_32x32x32_bf16 v[128:143], v[212:215], v[72:79], v216\n\t"
        "s_waitcnt lgkmcnt(2)\n\t"
        "ds_read_b128 v[204:207], v219 offset:29440\n\t"
        "v_exp_f32 v12, v12\n\t"
        "v_exp_f32 v13, v13\n\t"
        "v_exp_f32 v14, v14\n\t"
        "v_exp_f32 v15, v15\n\t"
        "v_mfma_f32_32x32x16_bf16 v[32:47], v[184:187], v[168:171], v[32:47]\n\t"
        "ds_read_b128 v[180:183], v217 offset:35840\n\t"
        "v_exp_f32 v16, v16\n\t"
        "v_exp_f32 v17, v17\n\t"
        "v_exp_f32 v18, v18\n\t"
        "v_exp_f32 v19, v19\n\t"
        "v_mfma_f32_32x32x16_bf16 v[48:63], v[184:187], v[172:175], v[48:63]\n\t"
        "v_exp_f32 v20, v20\n\t"
        "v_exp_f32 v21, v21\n\t"
        "v_exp_f32 v22, v22\n\t"
        "v_exp_f32 v23, v23\n\t"
        "v_smfmac_f32_32x32x32_bf16 v[144:159], v[208:211], v[80:87], v216\n\t"
        "v_exp_f32 v24, v24\n\t"
        "v_exp_f32 v25, v25\n\t"
        "v_exp_f32 v26, v26\n\t"
        "v_exp_f32 v27, v27\n\t"
        "v_smfmac_f32_32x32x32_bf16 v[144:159], v[212:215], v[88:95], v216\n\t"
        "s_waitcnt lgkmcnt(1)\n\t"
        "v_exp_f32 v28, v28\n\t"
        "v_exp_f32 v29, v29\n\t"
        "v_exp_f32 v30, v30\n\t"
        "v_exp_f32 v31, v31\n\t"
        "v_mfma_f32_32x32x4_2b_bf16 v[64:95], v[196:197], v[178:179], 0\n\t"
        "v_exp_f32 v32, v32\n\t"
        "v_exp_f32 v33, v33\n\t"
        "v_exp_f32 v34, v34\n\t"
        "v_exp_f32 v35, v35\n\t"
        "v_exp_f32 v36, v36\n\t"
        "v_exp_f32 v37, v37\n\t"
        "v_exp_f32 v38, v38\n\t"
        "v_exp_f32 v39, v39\n\t"
        "v_smfmac_f32_32x32x32_bf16 v[128:143], v[200:203], v[0:7], v216\n\t"
        "ds_read_b128 v[208:211], v219 offset:32256\n\t"
        "ds_read_b128 v[212:215], v219 offset:33024\n\t"
        "v_exp_f32 v40, v40\n\t"
        "v_exp_f32 v41, v41\n\t"
        "v_exp_f32 v42, v42\n\t"
        "v_exp_f32 v43, v43\n\t"
        "v_smfmac_f32_32x32x32_bf16 v[128:143], v[204:207], v[8:15], v216\n\t"
        "s_waitcnt lgkmcnt(2)\n\t"
        "ds_read_b128 v[184:187], v217 offset:36864\n\t"
        "v_exp_f32 v44, v44\n\t"
        "v_exp_f32 v45, v45\n\t"
        "v_exp_f32 v46, v46\n\t"
        "v_exp_f32 v47, v47\n\t"
        "v_mfma_f32_32x32x16_bf16 v[64:79], v[180:183], v[168:171], v[64:79]\n\t"
        "v_exp_f32 v48, v48\n\t"
        "v_exp_f32 v49, v49\n\t"
        "v_exp_f32 v50, v50\n\t"
        "v_exp_f32 v51, v51\n\t"
        "v_mfma_f32_32x32x16_bf16 v[80:95], v[180:183], v[172:175], v[80:95]\n\t"
        "v_exp_f32 v52, v52\n\t"
        "v_exp_f32 v53, v53\n\t"
        "v_exp_f32 v54, v54\n\t"
        "v_exp_f32 v55, v55\n\t"
        "v_smfmac_f32_32x32x32_bf16 v[144:159], v[200:203], v[16:23], v216\n\t"
        "v_exp_f32 v56, v56\n\t"
        "v_exp_f32 v57, v57\n\t"
        "v_exp_f32 v58, v58\n\t"
        "v_exp_f32 v59, v59\n\t"
        "v_smfmac_f32_32x32x32_bf16 v[144:159], v[204:207], v[24:31], v216\n\t"
        "s_waitcnt lgkmcnt(1)\n\t"
        "v_exp_f32 v60, v60\n\t"
        "v_exp_f32 v61, v61\n\t"
        "v_exp_f32 v62, v62\n\t"
        "v_exp_f32 v63, v63\n\t"
        "v_mfma_f32_32x32x4_2b_bf16 v[0:31], v[198:199], v[178:179], 0\n\t"
        "v_exp_f32 v64, v64\n\t"
        "v_exp_f32 v65, v65\n\t"
        "v_exp_f32 v66, v66\n\t"
        "v_exp_f32 v67, v67\n\t"
        "v_exp_f32 v68, v68\n\t"
        "v_exp_f32 v69, v69\n\t"
        "v_exp_f32 v70, v70\n\t"
        "v_exp_f32 v71, v71\n\t"
        "v_smfmac_f32_32x32x32_bf16 v[128:143], v[208:211], v[32:39], v216\n\t"
        "ds_read2st64_b64 v[188:191], v218 offset0:77 offset1:79\n\t"
        "ds_read_b128 v[200:203], v219 offset:34048\n\t"
        "v_exp_f32 v72, v72\n\t"
        "v_exp_f32 v73, v73\n\t"
        "v_exp_f32 v74, v74\n\t"
        "v_exp_f32 v75, v75\n\t"
        "v_smfmac_f32_32x32x32_bf16 v[128:143], v[212:215], v[40:47], v216\n\t"
        "s_waitcnt lgkmcnt(2)\n\t"
        "ds_read_b128 v[204:207], v219 offset:35072\n\t"
        "v_exp_f32 v76, v76\n\t"
        "v_exp_f32 v77, v77\n\t"
        "v_exp_f32 v78, v78\n\t"
        "v_exp_f32 v79, v79\n\t"
        "v_mfma_f32_32x32x16_bf16 v[0:15], v[184:187], v[168:171], v[0:15]\n\t"
        "ds_read_b128 v[180:183], v217 offset:41472\n\t"
        "v_exp_f32 v80, v80\n\t"
        "v_exp_f32 v81, v81\n\t"
        "v_exp_f32 v82, v82\n\t"
        "v_exp_f32 v83, v83\n\t"
        "v_mfma_f32_32x32x16_bf16 v[16:31], v[184:187], v[172:175], v[16:31]\n\t"
        "v_exp_f32 v84, v84\n\t"
        "v_exp_f32 v85, v85\n\t"
        "v_exp_f32 v86, v86\n\t"
        "v_exp_f32 v87, v87\n\t"
        "v_smfmac_f32_32x32x32_bf16 v[144:159], v[208:211], v[48:55], v216\n\t"
        "v_exp_f32 v88, v88\n\t"
        "v_exp_f32 v89, v89\n\t"
        "v_exp_f32 v90, v90\n\t"
        "v_exp_f32 v91, v91\n\t"
        "v_smfmac_f32_32x32x32_bf16 v[144:159], v[212:215], v[56:63], v216\n\t"
        "s_waitcnt lgkmcnt(1)\n\t"
        "v_exp_f32 v92, v92\n\t"
        "v_exp_f32 v93, v93\n\t"
        "v_exp_f32 v94, v94\n\t"
        "v_exp_f32 v95, v95\n\t"
        "v_mfma_f32_32x32x4_2b_bf16 v[32:63], v[188:189], v[178:179], 0\n\t"
        "v_exp_f32 v0, v0\n\t"
        "v_exp_f32 v1, v1\n\t"
        "v_exp_f32 v2, v2\n\t"
        "v_exp_f32 v3, v3\n\t"
        "v_exp_f32 v4, v4\n\t"
        "v_exp_f32 v5, v5\n\t"
        "v_exp_f32 v6, v6\n\t"
        "v_exp_f32 v7, v7\n\t"
        "v_smfmac_f32_32x32x32_bf16 v[128:143], v[200:203], v[64:71], v216\n\t"
        "ds_read_b128 v[208:211], v219 offset:37888\n\t"
        "ds_read_b128 v[212:215], v219 offset:38656\n\t"
        "v_exp_f32 v8, v8\n\t"
        "v_exp_f32 v9, v9\n\t"
        "v_exp_f32 v10, v10\n\t"
        "v_exp_f32 v11, v11\n\t"
        "v_smfmac_f32_32x32x32_bf16 v[128:143], v[204:207], v[72:79], v216\n\t"
        "s_waitcnt lgkmcnt(2)\n\t"
        "ds_read_b128 v[184:187], v217 offset:42496\n\t"
        "v_exp_f32 v12, v12\n\t"
        "v_exp_f32 v13, v13\n\t"
        "v_exp_f32 v14, v14\n\t"
        "v_exp_f32 v15, v15\n\t"
        "v_mfma_f32_32x32x16_bf16 v[32:47], v[180:183], v[168:171], v[32:47]\n\t"
        "v_exp_f32 v16, v16\n\t"
        "v_exp_f32 v17, v17\n\t"
        "v_exp_f32 v18, v18\n\t"
        "v_exp_f32 v19, v19\n\t"
        "v_mfma_f32_32x32x16_bf16 v[48:63], v[180:183], v[172:175], v[48:63]\n\t"
        "v_exp_f32 v20, v20\n\t"
        "v_exp_f32 v21, v21\n\t"
        "v_exp_f32 v22, v22\n\t"
        "v_exp_f32 v23, v23\n\t"
        "v_smfmac_f32_32x32x32_bf16 v[144:159], v[200:203], v[80:87], v216\n\t"
        "v_exp_f32 v24, v24\n\t"
        "v_exp_f32 v25, v25\n\t"
        "v_exp_f32 v26, v26\n\t"
        "v_exp_f32 v27, v27\n\t"
        "v_smfmac_f32_32x32x32_bf16 v[144:159], v[204:207], v[88:95], v216\n\t"
        "s_waitcnt lgkmcnt(1)\n\t"
        "v_exp_f32 v28, v28\n\t"
        "v_exp_f32 v29, v29\n\t"
        "v_exp_f32 v30, v30\n\t"
        "v_exp_f32 v31, v31\n\t"
        "v_mfma_f32_32x32x4_2b_bf16 v[64:95], v[190:191], v[178:179], 0\n\t"
        "v_exp_f32 v32, v32\n\t"
        "v_exp_f32 v33, v33\n\t"
        "v_exp_f32 v34, v34\n\t"
        "v_exp_f32 v35, v35\n\t"
        "v_exp_f32 v36, v36\n\t"
        "v_exp_f32 v37, v37\n\t"
        "v_exp_f32 v38, v38\n\t"
        "v_exp_f32 v39, v39\n\t"
        "v_smfmac_f32_32x32x32_bf16 v[128:143], v[208:211], v[0:7], v216\n\t"
        "ds_read2st64_b64 v[192:195], v218 offset0:88 offset1:90\n\t"
        "ds_read_b128 v[200:203], v219 offset:39680\n\t"
        "v_exp_f32 v40, v40\n\t"
        "v_exp_f32 v41, v41\n\t"
        "v_exp_f32 v42, v42\n\t"
        "v_exp_f32 v43, v43\n\t"
        "v_smfmac_f32_32x32x32_bf16 v[128:143], v[212:215], v[8:15], v216\n\t"
        "s_waitcnt lgkmcnt(2)\n\t"
        "ds_read_b128 v[204:207], v219 offset:40704\n\t"
        "v_exp_f32 v44, v44\n\t"
        "v_exp_f32 v45, v45\n\t"
        "v_exp_f32 v46, v46\n\t"
        "v_exp_f32 v47, v47\n\t"
        "v_mfma_f32_32x32x16_bf16 v[64:79], v[184:187], v[168:171], v[64:79]\n\t"
        "ds_read_b128 v[180:183], v217 offset:47104\n\t"
        "v_exp_f32 v48, v48\n\t"
        "v_exp_f32 v49, v49\n\t"
        "v_exp_f32 v50, v50\n\t"
        "v_exp_f32 v51, v51\n\t"
        "v_mfma_f32_32x32x16_bf16 v[80:95], v[184:187], v[172:175], v[80:95]\n\t"
        "v_exp_f32 v52, v52\n\t"
        "v_exp_f32 v53, v53\n\t"
        "v_exp_f32 v54, v54\n\t"
        "v_exp_f32 v55, v55\n\t"
        "v_smfmac_f32_32x32x32_bf16 v[144:159], v[208:211], v[16:23], v216\n\t"
        "v_exp_f32 v56, v56\n\t"
        "v_exp_f32 v57, v57\n\t"
        "v_exp_f32 v58, v58\n\t"
        "v_exp_f32 v59, v59\n\t"
        "v_smfmac_f32_32x32x32_bf16 v[144:159], v[212:215], v[24:31], v216\n\t"
        "s_waitcnt lgkmcnt(1)\n\t"
        "v_exp_f32 v60, v60\n\t"
        "v_exp_f32 v61, v61\n\t"
        "v_exp_f32 v62, v62\n\t"
        "v_exp_f32 v63, v63\n\t"
        "v_mfma_f32_32x32x4_2b_bf16 v[0:31], v[192:193], v[178:179], 0\n\t"
        "v_exp_f32 v64, v64\n\t"
        "v_exp_f32 v65, v65\n\t"
        "v_exp_f32 v66, v66\n\t"
        "v_exp_f32 v67, v67\n\t"
        "v_exp_f32 v68, v68\n\t"
        "v_exp_f32 v69, v69\n\t"
        "v_exp_f32 v70, v70\n\t"
        "v_exp_f32 v71, v71\n\t"
        "v_smfmac_f32_32x32x32_bf16 v[128:143], v[200:203], v[32:39], v216\n\t"
        "ds_read_b128 v[208:211], v219 offset:43520\n\t"
        "ds_read_b128 v[212:215], v219 offset:44288\n\t"
        "v_exp_f32 v72, v72\n\t"
        "v_exp_f32 v73, v73\n\t"
        "v_exp_f32 v74, v74\n\t"
        "v_exp_f32 v75, v75\n\t"
        "v_smfmac_f32_32x32x32_bf16 v[128:143], v[204:207], v[40:47], v216\n\t"
        "s_waitcnt lgkmcnt(2)\n\t"
        "ds_read_b128 v[184:187], v217 offset:48128\n\t"
        "v_exp_f32 v76, v76\n\t"
        "v_exp_f32 v77, v77\n\t"
        "v_exp_f32 v78, v78\n\t"
        "v_exp_f32 v79, v79\n\t"
        "v_mfma_f32_32x32x16_bf16 v[0:15], v[180:183], v[168:171], v[0:15]\n\t"
        "v_exp_f32 v80, v80\n\t"
        "v_exp_f32 v81, v81\n\t"
        "v_exp_f32 v82, v82\n\t"
        "v_exp_f32 v83, v83\n\t"
        "v_mfma_f32_32x32x16_bf16 v[16:31], v[180:183], v[172:175], v[16:31]\n\t"
        "v_exp_f32 v84, v84\n\t"
        "v_exp_f32 v85, v85\n\t"
        "v_exp_f32 v86, v86\n\t"
        "v_exp_f32 v87, v87\n\t"
        "v_smfmac_f32_32x32x32_bf16 v[144:159], v[200:203], v[48:55], v216\n\t"
        "v_mov_b32 v223, 5\n\t"
        "v_exp_f32 v88, v88\n\t"
        "v_exp_f32 v89, v89\n\t"
        "v_exp_f32 v90, v90\n\t"
        "v_exp_f32 v91, v91\n\t"
        "v_smfmac_f32_32x32x32_bf16 v[144:159], v[204:207], v[56:63], v216\n\t"
        "s_waitcnt lgkmcnt(1)\n\t"
        "v_exp_f32 v92, v92\n\t"
        "v_exp_f32 v93, v93\n\t"
        "v_exp_f32 v94, v94\n\t"
        "v_exp_f32 v95, v95\n\t"
        "v_mfma_f32_32x32x4_2b_bf16 v[32:63], v[194:195], v[178:179], 0\n\t"
        "v_exp_f32 v0, v0\n\t"
        "v_exp_f32 v1, v1\n\t"
        "v_exp_f32 v2, v2\n\t"
        "v_exp_f32 v3, v3\n\t"
        "v_exp_f32 v4, v4\n\t"
        "v_exp_f32 v5, v5\n\t"
        "v_exp_f32 v6, v6\n\t"
        "v_exp_f32 v7, v7\n\t"
        "v_smfmac_f32_32x32x32_bf16 v[128:143], v[208:211], v[64:71], v216\n\t"
        "ds_read_b128 v[200:203], v219 offset:45312\n\t"
        "v_exp_f32 v8, v8\n\t"
        "v_exp_f32 v9, v9\n\t"
        "v_exp_f32 v10, v10\n\t"
        "v_exp_f32 v11, v11\n\t"
        "v_smfmac_f32_32x32x32_bf16 v[128:143], v[212:215], v[72:79], v216\n\t"
        "s_waitcnt lgkmcnt(1)\n\t"
        "ds_read_b128 v[204:207], v219 offset:46336\n\t"
        "v_exp_f32 v12, v12\n\t"
        "v_exp_f32 v13, v13\n\t"
        "v_exp_f32 v14, v14\n\t"
        "v_exp_f32 v15, v15\n\t"
        "v_mfma_f32_32x32x16_bf16 v[32:47], v[184:187], v[168:171], v[32:47]\n\t"
        "v_exp_f32 v16, v16\n\t"
        "v_exp_f32 v17, v17\n\t"
        "v_exp_f32 v18, v18\n\t"
        "v_exp_f32 v19, v19\n\t"
        "v_mfma_f32_32x32x16_bf16 v[48:63], v[184:187], v[172:175], v[48:63]\n\t"
        "v_exp_f32 v20, v20\n\t"
        "v_exp_f32 v21, v21\n\t"
        "v_exp_f32 v22, v22\n\t"
        "v_exp_f32 v23, v23\n\t"
        "v_smfmac_f32_32x32x32_bf16 v[144:159], v[208:211], v[80:87], v216\n\t"
        "v_exp_f32 v24, v24\n\t"
        "v_exp_f32 v25, v25\n\t"
        "v_exp_f32 v26, v26\n\t"
        "v_exp_f32 v27, v27\n\t"
        "v_smfmac_f32_32x32x32_bf16 v[144:159], v[212:215], v[88:95], v216\n\t"
        "s_waitcnt lgkmcnt(0)\n\t"
        "v_exp_f32 v28, v28\n\t"
        "v_exp_f32 v29, v29\n\t"
        "v_exp_f32 v30, v30\n\t"
        "v_exp_f32 v31, v31\n\t"
        "v_smfmac_f32_32x32x32_bf16 v[128:143], v[200:203], v[0:7], v216\n\t"
        "ds_read_b128 v[208:211], v219 offset:49152\n\t"
        "v_exp_f32 v32, v32\n\t"
        "v_exp_f32 v33, v33\n\t"
        "v_exp_f32 v34, v34\n\t"
        "v_exp_f32 v35, v35\n\t"
        "v_smfmac_f32_32x32x32_bf16 v[128:143], v[204:207], v[8:15], v216\n\t"
        "ds_read_b128 v[212:215], v219 offset:49920\n\t"
        "v_exp_f32 v36, v36\n\t"
        "v_exp_f32 v37, v37\n\t"
        "v_exp_f32 v38, v38\n\t"
        "v_exp_f32 v39, v39\n\t"
        "v_smfmac_f32_32x32x32_bf16 v[144:159], v[200:203], v[16:23], v216\n\t"
        "v_exp_f32 v40, v40\n\t"
        "v_exp_f32 v41, v41\n\t"
        "v_exp_f32 v42, v42\n\t"
        "v_exp_f32 v43, v43\n\t"
        "v_smfmac_f32_32x32x32_bf16 v[144:159], v[204:207], v[24:31], v216\n\t"
        "s_waitcnt lgkmcnt(0)\n\t"
        "v_exp_f32 v44, v44\n\t"
        "v_exp_f32 v45, v45\n\t"
        "v_exp_f32 v46, v46\n\t"
        "v_smfmac_f32_32x32x32_bf16 v[128:143], v[208:211], v[32:39], v216\n\t"
        "v_exp_f32 v47, v47\n\t"
        "v_exp_f32 v48, v48\n\t"
        "v_exp_f32 v49, v49\n\t"
        "v_smfmac_f32_32x32x32_bf16 v[128:143], v[212:215], v[40:47], v216\n\t"
        "v_exp_f32 v50, v50\n\t"
        "v_exp_f32 v51, v51\n\t"
        "v_exp_f32 v52, v52\n\t"
        "v_exp_f32 v53, v53\n\t"
        "v_exp_f32 v54, v54\n\t"
        "v_exp_f32 v55, v55\n\t"
        "v_exp_f32 v56, v56\n\t"
        "v_exp_f32 v57, v57\n\t"
        "v_smfmac_f32_32x32x32_bf16 v[144:159], v[208:211], v[48:55], v216\n\t"
        "v_exp_f32 v58, v58\n\t"
        "v_exp_f32 v59, v59\n\t"
        "v_exp_f32 v60, v60\n\t"
        "v_exp_f32 v61, v61\n\t"
        "v_exp_f32 v62, v62\n\t"
        "v_exp_f32 v63, v63\n\t"
        "v_subrev_u32 v229, %[lo], v138\n\t"
        "v_rcp_f32 v138, v138\n\t"
        "v_smfmac_f32_32x32x32_bf16 v[144:159], v[212:215], v[56:63], v216\n\t"
        "ds_write_b32 v222, v223\n\t"
        "v_mul_f32 v138, v220, v138\n\t"
        "v_pk_mul_f32 v[128:129], v[128:129], v[138:139] op_sel_hi:[1,0]\n\t"
        "v_pk_mul_f32 v[130:131], v[130:131], v[138:139] op_sel_hi:[1,0]\n\t"
        "v_pk_mul_f32 v[132:133], v[132:133], v[138:139] op_sel_hi:[1,0]\n\t"
        "v_pk_mul_f32 v[134:135], v[134:135], v[138:139] op_sel_hi:[1,0]\n\t"
        "v_pk_mul_f32 v[136:137], v[136:137], v[138:139] op_sel_hi:[1,0]\n\t"
        "v_cvt_pk_bf16_f32 v140, v128, v129\n\t"
        "v_cvt_pk_bf16_f32 v141, v130, v131\n\t"
        "v_cvt_pk_bf16_f32 v142, v132, v133\n\t"
        "v_cvt_pk_bf16_f32 v143, v134, v135\n\t"
        "v_cvt_pk_bf16_f32 v226, v136, v137\n\t"
        "buffer_store_dwordx4 v[140:143], v221, %[srdws], 0 offen offset:2560 sc1\n\t"
        "v_subrev_u32 v230, %[lo], v154\n\t"
        "v_rcp_f32 v154, v154\n\t"
        "s_nop 0\n\t"
        "v_mul_f32 v154, v220, v154\n\t"
        "v_pk_mul_f32 v[144:145], v[144:145], v[154:155] op_sel_hi:[1,0]\n\t"
        "v_pk_mul_f32 v[146:147], v[146:147], v[154:155] op_sel_hi:[1,0]\n\t"
        "v_pk_mul_f32 v[148:149], v[148:149], v[154:155] op_sel_hi:[1,0]\n\t"
        "v_pk_mul_f32 v[150:151], v[150:151], v[154:155] op_sel_hi:[1,0]\n\t"
        "v_pk_mul_f32 v[152:153], v[152:153], v[154:155] op_sel_hi:[1,0]\n\t"
        "v_cvt_pk_bf16_f32 v156, v144, v145\n\t"
        "v_cvt_pk_bf16_f32 v157, v146, v147\n\t"
        "v_cvt_pk_bf16_f32 v158, v148, v149\n\t"
        "v_cvt_pk_bf16_f32 v159, v150, v151\n\t"
        "v_cvt_pk_bf16_f32 v227, v152, v153\n\t"
        "buffer_store_dwordx4 v[156:159], v221, %[srdws], 0 offen offset:3584 sc1\n\t"
        "buffer_store_dwordx2 v[226:227], v233, %[srdws], 0 offen offset:2560 sc1\n\t"
        "v_max_u32 v229, v229, v230\n\t"
        "v_cmp_lt_u32 %[bady], %[span], v229\n\t"
        "s_nop 1\n\t"
        : "=&{v[160:163]}"(q0), "=&{v[164:167]}"(q1), "=&{v[168:171]}"(q2), "=&{v[172:175]}"(q3), "=&{v[176:177]}"(r0), "=&{v[178:179]}"(r1),
          "+{v[200:215]}"(in), "+{v[216:231]}"(ab), "+{v[232:233]}"(cc), "+{s[68:69]}"(tag), [xp] "+s"(xp), [it] "=&s"(it), [t0] "=&s"(t0), [t1] "=&s"(t1), [badx] "=&s"(badx), [bady] "=&s"(bady)
        : [srdws] "s"(srdws), [isv] "s"(isv), [gsc] "s"(gsc), [ksc] "s"(ksc), [xstep] "s"(xstep), [xend] "s"(xend), [qslot] "s"(qslot),
          [done] "s"(done), [chsz] "s"(chsz), [lo] "s"(lo), [span] "s"(span)
        : "memory", "scc", "vcc", "s70", "s71", "v0","v1","v2","v3","v4","v5","v6","v7","v8","v9","v10","v11","v12","v13","v14","v15","v16","v17","v18","v19","v20","v21","v22","v23",
        "v24","v25","v26","v27","v28","v29","v30","v31","v32","v33","v34","v35","v36","v37","v38","v39","v40","v41","v42","v43","v44","v45","v46","v47",
        "v48","v49","v50","v51","v52","v53","v54","v55","v56","v57","v58","v59","v60","v61","v62","v63","v64","v65","v66","v67","v68","v69","v70","v71",
        "v72","v73","v74","v75","v76","v77","v78","v79","v80","v81","v82","v83","v84","v85","v86","v87","v88","v89","v90","v91","v92","v93","v94","v95",
        "v96","v97","v98","v99","v100","v101","v102","v103","v104","v105","v106","v107","v108","v109","v110","v111","v112","v113","v114","v115","v116","v117","v118","v119",
        "v120","v121","v122","v123","v124","v125","v126","v127","v128","v129","v130","v131","v132","v133","v134","v135","v136","v137","v138","v139","v140","v141","v142","v143",
        "v144","v145","v146","v147","v148","v149","v150","v151","v152","v153","v154","v155","v156","v157","v158","v159","v180","v181","v182","v183","v184","v185","v186","v187",
        "v188","v189","v190","v191","v192","v193","v194","v195","v196","v197","v198","v199");
      fx = ab[12];
      if (__builtin_expect((badx | bady) != 0, 0)) {
        const unsigned gfu = ab[4], ob = ab[5];
        const float gf = __uint_as_float(gfu);
        const unsigned oe = cc[1];
#pragma unroll 1
        for (int pr = 0; pr < 2; ++pr) {
          if ((pr ? bady : badx) == 0) continue;
          f32x16 oa, oc;
          slow_pair(oa, oc, pr ? q2 : q0, pr ? q3 : q1, pr ? r1 : r0, srdws, kvb, cc[0] >> 4);
          const float fa = gf / oa[10], fc = gf / oc[10];
          u32x4 ca, cb;
          for (int i = 0; i < 4; ++i) { ca[i] = pk(oa[2 * i] * fa, oa[2 * i + 1] * fa); cb[i] = pk(oc[2 * i] * fc, oc[2 * i + 1] * fc); }
          __builtin_amdgcn_raw_buffer_store_b128(ca, srdws, ob + pr * 2560, 0, 16);
          __builtin_amdgcn_raw_buffer_store_b128(cb, srdws, ob + pr * 2560 + 1024, 0, 16);
          const u32x2 ce = {pk(oa[8] * fa, oa[9] * fa), pk(oc[8] * fc, oc[9] * fc)};
          __builtin_amdgcn_raw_buffer_store_b64(ce, srdws, oe + pr * 2560, 0, 16);
        }
        if (badx != 0) {
          asm volatile("v_mov_b32 v0, s68\n\tv_mov_b32 v1, s69\n\ts_waitcnt vmcnt(0)\n\tbuffer_store_dwordx2 v[0:1], %0, %1, 0 offen sc1\n\ts_nop 1"
                       :: "v"(fx), "s"(srdws), "{s[68:69]}"(tag) : "memory", "v0", "v1");
        }
      }
    }
    {
      const unsigned mo = (w & 1) * 10240 + (w >> 1) * 5120, done = 163344 + 4 * w;
      unsigned soo = WS_OF + (tb * 4 + 2 * b + (w & 1)) * 81920 + (w >> 1) * 5120 + 2560, msk;
      const unsigned sa = L_T4 + (w >> 1) * 5120 + (w & 1) * 2048, sb = L_T4 + w * 10240, sc = L_OX + (w >> 1) * 12288 + (w & 1) * 5376;
      const unsigned sd = L_OX + w * 20480 + (w >> 1) * 10240 + (w & 1) * 128, se = L_RED + 32768 + w * 4096;
      const unsigned sf = (tb * 128 + 64 * b + 16 * w) * 1280 + 1024 + 32 * hg, sg = WS_FO + ((tb * 16 + (w >> 1)) * 4 + 2 * b + (w & 1)) * 256 + 128;
      const unsigned amask = b ? 0xffffff0fu : 0xffffff00u;
      const unsigned dtb = (w >> 1) ? 0u - 10240u : 10240u, dey = (w >> 1) ? 122880 - 40960 - L_OX : 0u - L_OX;
      asm volatile(
        "v_mov_b32 v236, v232\n\t"
        "v_add_u32 v242, %[sa], v232\n\t"
        "v_add_u32 v243, %[sb], v232\n\t"
        "v_add_u32 v210, 8, v243\n\t"
        "v_not_b32 v204, s68\n\t"
        "ds_read_b128 v[0:3], v242 offset:0\n\t"
        "ds_read_b128 v[4:7], v242 offset:10240\n\t"
        "ds_read_b128 v[8:11], v242 offset:20480\n\t"
        "ds_read_b128 v[12:15], v242 offset:30720\n\t"
        "ds_read2st64_b64 v[16:19], v243 offset0:8 offset1:18\n\t"
        "ds_read_b128 v[20:23], v242 offset:1024\n\t"
        "ds_read_b128 v[24:27], v242 offset:11264\n\t"
        "ds_read_b128 v[28:31], v242 offset:21504\n\t"
        "ds_read_b128 v[32:35], v242 offset:31744\n\t"
        "ds_read2st64_b64 v[36:39], v210 offset0:8 offset1:18\n\t"
        "v_mov_b32 v207, %[done]\n\t"
        "v_mov_b32 v208, s68\n\t"
        "v_mov_b32 v209, s69\n\t"
        "v_bfe_u32 v211, v236, 4, 3\n\t"
        "v_lshlrev_b32 v211, 2, v211\n\t"
        "v_add_u32 v211, 163328, v211\n\t"
        "s_waitcnt lgkmcnt(0)\n\t"
        "ds_write_b32 v207, v207\n\t"
        "v_add_u32 v237, 0x80, v228\n\t"
        "v_lshl_add_u32 v238, v236, 7, %[sg]\n\t"
        "v_cmp_gt_u32 vcc, 0x80, v236\n\t"
        "v_and_b32 v200, 0x100, v236\n\t"
        "v_and_b32 v201, 0x1f0, v236\n\t"
        "v_cndmask_b32 v238, 2.0, v238, vcc\n\t"
        "v_add_u32 v200, v200, v201\n\t"
        "v_lshrrev_b32 v201, 9, v236\n\t"
        "v_lshl_add_u32 v232, v201, 10, v200\n\t"
        "v_add_u32 v232, %[sc], v232\n\t"
        "v_xor_b32 v240, 0x100, v232\n\t"
        "v_mul_u32_u24 v202, 0x1400, v201\n\t"
        "v_lshrrev_b32 v200, 1, v200\n\t"
        "v_add3_u32 v234, v202, v200, %[sd]\n\t"
        "v_xor_b32 v241, 0x80, v234\n\t"
        "v_mul_u32_u24 v235, 9728, v201\n\t"
        "v_add_u32 v235, v235, v236\n\t"
        "v_add_u32 v235, 0x800, v235\n\t"
        "v_add_u32 v248, %[se], v236\n\t"
        "v_xor_b32 v249, 0x1400, v248\n\t"
        "v_xor_b32 v250, 0x2800, v248\n\t"
        "v_xor_b32 v251, 0x3c00, v248\n\t"
        "v_and_b32 v200, 0xf0, v236\n\t"
        "v_mul_u32_u24 v200, 0x50, v200\n\t"
        "v_lshrrev_b32 v201, 8, v236\n\t"
        "v_lshl_add_u32 v233, v201, 3, v200\n\t"
        "v_add_u32 v233, %[sf], v233\n\t"
        "v_add_u32 v244, %[dtb], v232\n\t"
        "v_add_u32 v245, %[dtb], v240\n\t"
        "v_add_u32 v246, %[dtb], v234\n\t"
        "v_add_u32 v247, %[dtb], v241\n\t"
        "s_mov_b32 exec_lo, %[amask]\n\t"
        "buffer_load_dwordx2 v[204:205], v238, %[srdws], 0 offen sc1\n\t"
        "s_mov_b32 exec_lo, -1\n\t"
        "s_movk_i32 %[msk], 0xff\n\t"
        "s_waitcnt vmcnt(1)\n\t"
        "buffer_store_dwordx2 v[208:209], v237, %[srdws], 0 offen sc1\n\t"
        "s_mov_b32 %[t1], 1048576\n\t"
        "5:\n\t"
        "ds_read_b32 v200, v211 offset:0\n\t"
        "s_waitcnt lgkmcnt(0)\n\t"
        "v_cmp_gt_u32 vcc, 6, v200\n\t"
        "s_cmp_eq_u64 vcc, 0\n\t"
        "s_cbranch_scc1 6f\n\t"
        "s_sleep 1\n\t"
        "s_sub_u32 %[t1], %[t1], 1\n\t"
        "s_cmp_lg_u32 %[t1], 0\n\t"
        "s_cbranch_scc1 5b\n\t"
        "6:\n\t"
        "s_waitcnt vmcnt(1)\n\t"
        "v_cmp_ne_u64 vcc, s[68:69], v[204:205]\n\t"
        "s_and_b32 vcc_lo, vcc_lo, 0xf\n\t"
        "s_cbranch_scc1 7f\n\t"
        "s_add_u32 m0, %[mo], 0\n\t"
        "s_add_u32 %[t0], %[soo], 8192\n\t"
        "buffer_load_dwordx4 v236, %[srdws], %[soo] offen sc1 lds\n\t"
        "buffer_load_dwordx4 v236, %[srdws], %[soo] offen offset:1024 sc1 lds\n\t"
        "buffer_load_dwordx4 v236, %[srdws], %[t0] offen offset:2048 sc1 lds\n\t"
        "buffer_load_dwordx4 v236, %[srdws], %[t0] offen offset:3072 sc1 lds\n\t"
        "s_add_u32 m0, %[mo], 4096\n\t"
        "s_nop 0\n\t"
        "buffer_load_dwordx4 v235, %[srdws], %[soo] offen sc1 lds\n\t"
        "s_add_u32 %[soo], %[soo], 20480\n\t"
        "s_add_u32 m0, %[mo], 20480\n\t"
        "s_add_u32 %[t0], %[soo], 8192\n\t"
        "buffer_load_dwordx4 v236, %[srdws], %[soo] offen sc1 lds\n\t"
        "buffer_load_dwordx4 v236, %[srdws], %[soo] offen offset:1024 sc1 lds\n\t"
        "buffer_load_dwordx4 v236, %[srdws], %[t0] offen offset:2048 sc1 lds\n\t"
        "buffer_load_dwordx4 v236, %[srdws], %[t0] offen offset:3072 sc1 lds\n\t"
        "s_add_u32 m0, %[mo], 24576\n\t"
        "s_nop 0\n\t"
        "buffer_load_dwordx4 v235, %[srdws], %[soo] offen sc1 lds\n\t"
        "s_add_u32 %[soo], %[soo], 20480\n\t"
        "s_mov_b32 %[msk], 0xf0\n\t"
        "7:\n\t"
        "ds_read_b128 v[64:67], v232 offset:0\n\t"
        "ds_read_b128 v[68:71], v240 offset:0\n\t"
        "ds_read_b128 v[72:75], v244 offset:0\n\t"
        "ds_read_b128 v[76:79], v245 offset:0\n\t"
        "ds_read_b128 v[80:83], v232 offset:20480\n\t"
        "ds_read_b128 v[84:87], v240 offset:20480\n\t"
        "ds_read_b128 v[88:91], v244 offset:20480\n\t"
        "ds_read_b128 v[92:95], v245 offset:20480\n\t"
        "v_add_u32 v232, 0xa000, v232\n\t"
        "v_add_u32 v240, 0xa000, v240\n\t"
        "v_add_u32 v244, 0xa000, v244\n\t"
        "v_add_u32 v245, 0xa000, v245\n\t"
        "ds_read_b128 v[96:99], v232 offset:0\n\t"
        "ds_read_b128 v[100:103], v240 offset:0\n\t"
        "ds_read_b128 v[104:107], v244 offset:0\n\t"
        "ds_read_b128 v[108:111], v245 offset:0\n\t"
        "s_waitcnt lgkmcnt(11)\n\t"
        "v_mfma_f32_16x16x32_bf16 v[160:163], v[0:3], v[64:67], 0\n\t"
        "ds_read_b128 v[112:115], v232 offset:20480\n\t"
        "s_waitcnt lgkmcnt(11)\n\t"
        "v_mfma_f32_16x16x32_bf16 v[164:167], v[0:3], v[68:71], 0\n\t"
        "ds_read_b128 v[116:119], v240 offset:20480\n\t"
        "s_waitcnt lgkmcnt(11)\n\t"
        "v_mfma_f32_16x16x32_bf16 v[168:171], v[0:3], v[72:75], 0\n\t"
        "ds_read_b128 v[120:123], v244 offset:20480\n\t"
        "s_waitcnt lgkmcnt(11)\n\t"
        "v_mfma_f32_16x16x32_bf16 v[172:175], v[0:3], v[76:79], 0\n\t"
        "ds_read_b128 v[124:127], v245 offset:20480\n\t"
        "s_waitcnt lgkmcnt(11)\n\t"
        "v_mfma_f32_16x16x32_bf16 v[160:163], v[4:7], v[80:83], v[160:163]\n\t"
        "ds_read2st64_b64 v[128:131], v234 offset0:8 offset1:9\n\t"
        "s_waitcnt lgkmcnt(11)\n\t"
        "v_mfma_f32_16x16x32_bf16 v[164:167], v[4:7], v[84:87], v[164:167]\n\t"
        "ds_read2st64_b64 v[132:135], v241 offset0:8 offset1:9\n\t"
        "s_waitcnt lgkmcnt(11)\n\t"
        "v_mfma_f32_16x16x32_bf16 v[168:171], v[4:7], v[88:91], v[168:171]\n\t"
        "ds_read2st64_b64 v[136:139], v246 offset0:8 offset1:9\n\t"
        "buffer_load_dwordx2 v[204:205], v238, %[srdws], 0 offen sc1\n\t"
        "s_waitcnt lgkmcnt(11)\n\t"
        "v_mfma_f32_16x16x32_bf16 v[172:175], v[4:7], v[92:95], v[172:175]\n\t"
        "ds_read2st64_b64 v[140:143], v247 offset0:8 offset1:9\n\t"
        "s_waitcnt lgkmcnt(11)\n\t"
        "v_mfma_f32_16x16x32_bf16 v[160:163], v[8:11], v[96:99], v[160:163]\n\t"
        "s_waitcnt lgkmcnt(10)\n\t"
        "v_mfma_f32_16x16x32_bf16 v[164:167], v[8:11], v[100:103], v[164:167]\n\t"
        "s_waitcnt lgkmcnt(9)\n\t"
        "v_mfma_f32_16x16x32_bf16 v[168:171], v[8:11], v[104:107], v[168:171]\n\t"
        "s_waitcnt lgkmcnt(8)\n\t"
        "v_mfma_f32_16x16x32_bf16 v[172:175], v[8:11], v[108:111], v[172:175]\n\t"
        "s_waitcnt lgkmcnt(7)\n\t"
        "v_mfma_f32_16x16x32_bf16 v[160:163], v[12:15], v[112:115], v[160:163]\n\t"
        "s_waitcnt lgkmcnt(6)\n\t"
        "v_mfma_f32_16x16x32_bf16 v[164:167], v[12:15], v[116:119], v[164:167]\n\t"
        "s_waitcnt lgkmcnt(5)\n\t"
        "v_mfma_f32_16x16x32_bf16 v[168:171], v[12:15], v[120:123], v[168:171]\n\t"
        "s_waitcnt lgkmcnt(4)\n\t"
        "v_mfma_f32_16x16x32_bf16 v[172:175], v[12:15], v[124:127], v[172:175]\n\t"
        "s_waitcnt lgkmcnt(3)\n\t"
        "v_mfma_f32_16x16x32_bf16 v[160:163], v[16:19], v[128:131], v[160:163]\n\t"
        "s_waitcnt lgkmcnt(2)\n\t"
        "v_mfma_f32_16x16x32_bf16 v[164:167], v[16:19], v[132:135], v[164:167]\n\t"
        "s_waitcnt lgkmcnt(1)\n\t"
        "v_mfma_f32_16x16x32_bf16 v[168:171], v[16:19], v[136:139], v[168:171]\n\t"
        "s_waitcnt lgkmcnt(0)\n\t"
        "v_mfma_f32_16x16x32_bf16 v[172:175], v[16:19], v[140:143], v[172:175]\n\t"
        "v_add_u32 v234, %[dey], v234\n\t"
        "v_add_u32 v241, %[dey], v241\n\t"
        "v_add_u32 v246, %[dey], v246\n\t"
        "v_add_u32 v247, %[dey], v247\n\t"
        "s_mov_b32 %[t1], 65536\n\t"
        "s_branch 11f\n\t"
        "12:\n\t"
        "s_and_b32 vcc_hi, vcc_lo, 0xf\n\t"
        "s_cbranch_scc1 3f\n\t"
        "s_add_u32 m0, %[mo], 0\n\t"
        "s_add_u32 %[t0], %[soo], 8192\n\t"
        "buffer_load_dwordx4 v236, %[srdws], %[soo] offen sc1 lds\n\t"
        "buffer_load_dwordx4 v236, %[srdws], %[soo] offen offset:1024 sc1 lds\n\t"
        "buffer_load_dwordx4 v236, %[srdws], %[t0] offen offset:2048 sc1 lds\n\t"
        "buffer_load_dwordx4 v236, %[srdws], %[t0] offen offset:3072 sc1 lds\n\t"
        "s_add_u32 m0, %[mo], 4096\n\t"
        "s_nop 0\n\t"
        "buffer_load_dwordx4 v235, %[srdws], %[soo] offen sc1 lds\n\t"
        "s_add_u32 %[soo], %[soo], 20480\n\t"
        "s_add_u32 m0, %[mo], 20480\n\t"
        "s_add_u32 %[t0], %[soo], 8192\n\t"
        "buffer_load_dwordx4 v236, %[srdws], %[soo] offen sc1 lds\n\t"
        "buffer_load_dwordx4 v236, %[srdws], %[soo] offen offset:1024 sc1 lds\n\t"
        "buffer_load_dwordx4 v236, %[srdws], %[t0] offen offset:2048 sc1 lds\n\t"
        "buffer_load_dwordx4 v236, %[srdws], %[t0] offen offset:3072 sc1 lds\n\t"
        "s_add_u32 m0, %[mo], 24576\n\t"
        "s_nop 0\n\t"
        "buffer_load_dwordx4 v235, %[srdws], %[soo] offen sc1 lds\n\t"
        "s_add_u32 %[soo], %[soo], 20480\n\t"
        "s_mov_b32 %[msk], 0xf0\n\t"
        "s_branch 8f\n\t"
        "3:\n\t"
        "buffer_load_dwordx2 v[204:205], v238, %[srdws], 0 offen sc1\n\t"
        "11:\n\t"
        "s_waitcnt vmcnt(0)\n\t"
        "v_cmp_ne_u64 vcc, s[68:69], v[204:205]\n\t"
        "s_sub_u32 %[t1], %[t1], 1\n\t"
        "s_cselect_b32 vcc_lo, 0, vcc_lo\n\t"
        "s_bitcmp1_b32 %[msk], 0\n\t"
        "s_cbranch_scc1 12b\n\t"
        "8:\n\t"
        "s_and_b32 vcc_hi, vcc_lo, 0xf0\n\t"
        "s_cbranch_scc1 3b\n\t"
        "s_add_u32 m0, %[mo], 122880\n\t"
        "s_add_u32 %[t0], %[soo], 8192\n\t"
        "buffer_load_dwordx4 v236, %[srdws], %[soo] offen sc1 lds\n\t"
        "buffer_load_dwordx4 v236, %[srdws], %[soo] offen offset:1024 sc1 lds\n\t"
        "buffer_load_dwordx4 v236, %[srdws], %[t0] offen offset:2048 sc1 lds\n\t"
        "s_waitcnt vmcnt(8)\n\t"
        "s_barrier\n\t"
        "buffer_load_dwordx4 v236, %[srdws], %[t0] offen offset:3072 sc1 lds\n\t"
        "s_add_u32 m0, %[mo], 126976\n\t"
        "s_nop 0\n\t"
        "buffer_load_dwordx4 v235, %[srdws], %[soo] offen sc1 lds\n\t"
        "s_add_u32 %[soo], %[soo], 20480\n\t"
        "s_waitcnt vmcnt(5)\n\t"
        "s_barrier\n\t"
        "v_add_u32 v232, 0xfffec000, v232\n\t"
        "v_add_u32 v240, 0xfffec000, v240\n\t"
        "v_add_u32 v244, 0xfffec000, v244\n\t"
        "v_add_u32 v245, 0xfffec000, v245\n\t"
        "ds_read_b128 v[64:67], v240 offset:0\n\t"
        "ds_read_b128 v[68:71], v240 offset:20480\n\t"
        "ds_read_b128 v[72:75], v244 offset:0\n\t"
        "ds_read_b128 v[76:79], v244 offset:20480\n\t"
        "ds_read_b128 v[80:83], v245 offset:0\n\t"
        "ds_read_b128 v[84:87], v245 offset:20480\n\t"
        "ds_read_b128 v[88:91], v232 offset:0\n\t"
        "ds_read_b128 v[92:95], v232 offset:20480\n\t"
        "s_add_u32 m0, %[mo], 143360\n\t"
        "s_add_u32 %[t0], %[soo], 8192\n\t"
        "buffer_load_dwordx4 v236, %[srdws], %[soo] offen sc1 lds\n\t"
        "buffer_load_dwordx4 v236, %[srdws], %[soo] offen offset:1024 sc1 lds\n\t"
        "buffer_load_dwordx4 v236, %[srdws], %[t0] offen offset:2048 sc1 lds\n\t"
        "buffer_load_dwordx4 v236, %[srdws], %[t0] offen offset:3072 sc1 lds\n\t"
        "s_add_u32 m0, %[mo], 147456\n\t"
        "s_nop 0\n\t"
        "buffer_load_dwordx4 v235, %[srdws], %[soo] offen sc1 lds\n\t"
        "s_add_u32 %[soo], %[soo], 20480\n\t"
        "s_waitcnt lgkmcnt(7)\n\t"
        "v_mfma_f32_16x16x32_bf16 v[164:167], v[20:23], v[64:67], v[164:167]\n\t"
        "s_waitcnt lgkmcnt(6)\n\t"
        "v_mfma_f32_16x16x32_bf16 v[164:167], v[24:27], v[68:71], v[164:167]\n\t"
        "s_waitcnt lgkmcnt(5)\n\t"
        "v_mfma_f32_16x16x32_bf16 v[168:171], v[20:23], v[72:75], v[168:171]\n\t"
        "s_waitcnt lgkmcnt(4)\n\t"
        "v_mfma_f32_16x16x32_bf16 v[168:171], v[24:27], v[76:79], v[168:171]\n\t"
        "s_waitcnt lgkmcnt(3)\n\t"
        "v_mfma_f32_16x16x32_bf16 v[172:175], v[20:23], v[80:83], v[172:175]\n\t"
        "s_waitcnt lgkmcnt(2)\n\t"
        "v_mfma_f32_16x16x32_bf16 v[172:175], v[24:27], v[84:87], v[172:175]\n\t"
        "s_waitcnt lgkmcnt(1)\n\t"
        "v_mfma_f32_16x16x32_bf16 v[160:163], v[20:23], v[88:91], v[160:163]\n\t"
        "s_waitcnt lgkmcnt(0)\n\t"
        "v_mfma_f32_16x16x32_bf16 v[160:163], v[24:27], v[92:95], v[160:163]\n\t"
        "s_waitcnt vmcnt(5)\n\t"
        "s_barrier\n\t"
        "s_waitcnt vmcnt(0)\n\t"
        "s_barrier\n\t"
        "v_add_u32 v232, 0x1e000, v232\n\t"
        "v_add_u32 v240, 0x1e000, v240\n\t"
        "v_add_u32 v244, 0x1e000, v244\n\t"
        "v_add_u32 v245, 0x1e000, v245\n\t"
        "ds_read_b128 v[64:67], v240 offset:0\n\t"
        "ds_read_b128 v[68:71], v240 offset:20480\n\t"
        "ds_read2st64_b64 v[72:75], v241 offset0:8 offset1:9\n\t"
        "ds_read_b128 v[76:79], v244 offset:0\n\t"
        "ds_read_b128 v[80:83], v244 offset:20480\n\t"
        "ds_read2st64_b64 v[84:87], v246 offset0:8 offset1:9\n\t"
        "ds_read_b128 v[88:91], v245 offset:0\n\t"
        "ds_read_b128 v[92:95], v245 offset:20480\n\t"
        "ds_read2st64_b64 v[96:99], v247 offset0:8 offset1:9\n\t"
        "ds_read_b128 v[100:103], v232 offset:0\n\t"
        "ds_read_b128 v[104:107], v232 offset:20480\n\t"
        "ds_read2st64_b64 v[108:111], v234 offset0:8 offset1:9\n\t"
        "s_waitcnt lgkmcnt(11)\n\t"
        "v_mfma_f32_16x16x32_bf16 v[164:167], v[28:31], v[64:67], v[164:167]\n\t"
        "s_waitcnt lgkmcnt(10)\n\t"
        "v_mfma_f32_16x16x32_bf16 v[164:167], v[32:35], v[68:71], v[164:167]\n\t"
        "s_waitcnt lgkmcnt(9)\n\t"
        "v_mfma_f32_16x16x32_bf16 v[164:167], v[36:39], v[72:75], v[164:167]\n\t"
        "s_waitcnt lgkmcnt(8)\n\t"
        "v_mfma_f32_16x16x32_bf16 v[168:171], v[28:31], v[76:79], v[168:171]\n\t"
        "s_waitcnt lgkmcnt(7)\n\t"
        "v_mfma_f32_16x16x32_bf16 v[168:171], v[32:35], v[80:83], v[168:171]\n\t"
        "s_waitcnt lgkmcnt(6)\n\t"
        "v_mfma_f32_16x16x32_bf16 v[168:171], v[36:39], v[84:87], v[168:171]\n\t"
        "s_waitcnt lgkmcnt(5)\n\t"
        "v_mfma_f32_16x16x32_bf16 v[172:175], v[28:31], v[88:91], v[172:175]\n\t"
        "s_waitcnt lgkmcnt(4)\n\t"
        "v_mfma_f32_16x16x32_bf16 v[172:175], v[32:35], v[92:95], v[172:175]\n\t"
        "s_waitcnt lgkmcnt(3)\n\t"
        "v_mfma_f32_16x16x32_bf16 v[172:175], v[36:39], v[96:99], v[172:175]\n\t"
        "s_waitcnt lgkmcnt(2)\n\t"
        "v_mfma_f32_16x16x32_bf16 v[160:163], v[28:31], v[100:103], v[160:163]\n\t"
        "s_waitcnt lgkmcnt(1)\n\t"
        "v_mfma_f32_16x16x32_bf16 v[160:163], v[32:35], v[104:107], v[160:163]\n\t"
        "s_waitcnt lgkmcnt(0)\n\t"
        "v_mfma_f32_16x16x32_bf16 v[160:163], v[36:39], v[108:111], v[160:163]\n\t"
        "s_nop 3\n\t"
        "ds_write_b128 v249, v[164:167]\n\t"
        "ds_write_b128 v250, v[168:171]\n\t"
        "ds_write_b128 v251, v[172:175]\n\t"
        "s_waitcnt lgkmcnt(0)\n\t"
        "s_barrier\n\t"
        "ds_read_b128 v[68:71], v248 offset:1024\n\t"
        "ds_read_b128 v[72:75], v248 offset:2048\n\t"
        "ds_read_b128 v[76:79], v248 offset:3072\n\t"
        "s_waitcnt lgkmcnt(2)\n\t"
        "v_pk_add_f32 v[160:161], v[160:161], v[68:69]\n\t"
        "v_pk_add_f32 v[162:163], v[162:163], v[70:71]\n\t"
        "s_waitcnt lgkmcnt(1)\n\t"
        "v_pk_add_f32 v[160:161], v[160:161], v[72:73]\n\t"
        "v_pk_add_f32 v[162:163], v[162:163], v[74:75]\n\t"
        "s_waitcnt lgkmcnt(0)\n\t"
        "v_pk_add_f32 v[160:161], v[160:161], v[76:77]\n\t"
        "v_pk_add_f32 v[162:163], v[162:163], v[78:79]\n\t"
        "v_cvt_pk_bf16_f32 v164, v160, v161\n\t"
        "v_cvt_pk_bf16_f32 v165, v162, v163\n\t"
        "buffer_store_dwordx2 v[164:165], v233, %[srdout], 0 offen nt\n\t"
        : "+{v[232:233]}"(cc), [soo] "+s"(soo), [t0] "=&s"(t0), [t1] "=&s"(t1), [msk] "=&s"(msk)
        : [srdws] "s"(srdws), [mo] "s"(mo), [done] "s"(done), [dtb] "s"(dtb), [amask] "s"(amask), [dey] "s"(dey), [srdout] "s"(srdout), "{s[68:69]}"(tag),
          "{v228}"(fx), [sa] "s"(sa), [sb] "s"(sb), [sc] "s"(sc), [sd] "s"(sd), [se] "s"(se), [sf] "s"(sf), [sg] "s"(sg)
        : "memory", "scc", "vcc", "v0","v1","v2","v3","v4","v5","v6","v7","v8","v9","v10","v11","v12","v13","v14","v15","v16","v17","v18","v19","v20","v21","v22","v23",
        "v24","v25","v26","v27","v28","v29","v30","v31","v32","v33","v34","v35","v36","v37","v38","v39","v40","v41","v42","v43","v44","v45","v46","v47",
        "v48","v49","v50","v51","v52","v53","v54","v55","v56","v57","v58","v59","v60","v61","v62","v63","v64","v65","v66","v67","v68","v69","v70","v71",
        "v72","v73","v74","v75","v76","v77","v78","v79","v80","v81","v82","v83","v84","v85","v86","v87","v88","v89","v90","v91","v92","v93","v94","v95",
        "v96","v97","v98","v99","v100","v101","v102","v103","v104","v105","v106","v107","v108","v109","v110","v111","v112","v113","v114","v115","v116","v117","v118","v119",
        "v120","v121","v122","v123","v124","v125","v126","v127","v128","v129","v130","v131","v132","v133","v134","v135","v136","v137","v138","v139","v140","v141","v142","v143",
        "v144","v145","v146","v147","v148","v149","v150","v151","v152","v153","v154","v155","v156","v157","v158","v159","v160","v161","v162","v163","v164","v165","v166","v167",
        "v168","v169","v170","v171","v172","v173","v174","v175","v176","v177","v178","v179","v180","v181","v182","v183","v184","v185","v186","v187","v188","v189","v190","v191",
        "v192","v193","v194","v195","v196","v197","v198","v199","v200","v201","v202","v203","v204","v205","v206","v207","v208","v209","v210","v211","v234","v235","v236","v237",
        "v238","v239","v240","v241","v242","v243","v244","v245","v246","v247","v248","v249","v250","v251");
    }
    __builtin_amdgcn_endpgm();
  }
}

extern "C" void kernel_launch(void* const* d_in, const int* in_sizes, int n_in, void* d_out, int out_size, void* d_ws, size_t ws_size,
                              hipStream_t stream) {
  mqa_kernel<<<256, 512, 0, stream>>>((const char*)d_in[0], (const char*)d_in[1], (const char*)d_in[2], (const char*)d_in[3], (const char*)d_in[4],
                                      (const char*)d_in[6], (const char*)d_in[7], (const char*)d_in[8], (const char*)d_in[9], (const char*)d_in[10],
                                      (char*)d_out, (char*)d_ws);
}
```
